```python
import torch

def _softmax(x, axis=-1):
    return torch.softmax(x, dim=axis)
def _gelu(x):
    return torch.nn.functional.gelu(x, approximate="tanh")
def _silu(x):
    return torch.nn.functional.silu(x)
def _sigmoid(x):
    return torch.sigmoid(x)
def _layernorm(x, axis=-1, eps=1e-5):
    m = x.mean(dim=axis, keepdim=True)
    v = x.var(dim=axis, keepdim=True, unbiased=False)
    return (x - m) / torch.sqrt(v + eps)
def _scatter_add(tgt, idx, src, axis=0):
    return tgt.clone().index_add_(axis, idx, src)
def _segment_sum(src, seg, n):
    out = torch.zeros((n,) + src.shape[1:], dtype=src.dtype)
    return out.index_add_(0, seg, src)
def _pad(x, pw):
    flat = tuple(v for pair in reversed(pw) for v in pair)
    return torch.nn.functional.pad(x, flat)

kernel_name = 'standalone__deep_cross_wide_deep__recsys'

def setup_inputs():

    _w = (lambda a: (lambda t: t.to(torch.bfloat16) if t.is_floating_point() else t)(torch.as_tensor(a)))
    B, F, D = 4096, 26, 128
    V, Nd = min(100000, 100000), 13
    Din = F * D
    H = 4 * D
    return {
        "idx": _w((lambda lo, hi, *s: torch.randint(lo, hi, s))(0, V, B, F).to(torch.int64)),
        "table": _w((lambda *s: torch.randn(*s, dtype=torch.bfloat16) * 1.6)(V, D) * 0.05),
        "x_wide": _w((lambda *s: torch.randn(*s, dtype=torch.bfloat16) * 0.8)(B, Nd)),
        "w_wide": _w((lambda *s: torch.randn(*s, dtype=torch.bfloat16) * 0.8)(Nd) * 0.05),
        "b_wide": _w((lambda *s: torch.randn(*s, dtype=torch.bfloat16) * 0.8)(1) * 0.01),
        "W1": _w((lambda *s: torch.randn(*s, dtype=torch.bfloat16) * 1.6)(Din, H) * (2.0 / Din) ** 0.5),
        "b1": _w((lambda *s: torch.randn(*s, dtype=torch.bfloat16) * 1.6)(H) * 0.01),
        "W2": _w((lambda *s: torch.randn(*s, dtype=torch.bfloat16) * 1.6)(H) * (1.0 / H) ** 0.5),
        "b2": _w((lambda *s: torch.randn(*s, dtype=torch.bfloat16) * 1.6)(1) * 0.01),
    }

def reference(idx, table, x_wide, w_wide, b_wide, W1, b1, W2, b2):
    emb = table[idx]
    deep_in = emb.reshape(emb.shape[0], -1)
    h = deep_in @ W1 + b1
    h = h * (lambda x, dt: x.to(dt))(h > 0, h.dtype)
    deep_logit = h @ W2 + b2
    wide_logit = x_wide @ w_wide + b_wide
    return _sigmoid(wide_logit + deep_logit)
```

```cpp
#include <hip/hip_runtime.h>
#include <hip/hip_bf16.h>
#include <stdint.h>

typedef unsigned short u16;
__device__ __forceinline__ float bf2f(u16 x){ return __uint_as_float(((unsigned)x)<<16); }
__device__ __forceinline__ float rbf(float x){
  unsigned u = __float_as_uint(x);
  u = (u + 0x7FFFu + ((u>>16)&1u)) & 0xFFFF0000u;
  return __uint_as_float(u);
}

constexpr int B=4096, F=26, D=128, ND=13, DIN=F*D, H=512;

__global__ __launch_bounds__(256) void naive_kernel(const int* __restrict__ idx, const u16* __restrict__ table,
    const u16* __restrict__ xw, const u16* __restrict__ ww, const u16* __restrict__ bw,
    const u16* __restrict__ W1, const u16* __restrict__ b1, const u16* __restrict__ W2, const u16* __restrict__ b2,
    u16* __restrict__ out) {
  __shared__ float a[4][DIN];
  __shared__ float red[4][256];
  int r0 = blockIdx.x*4;
  for (int i = threadIdx.x; i < 4*DIN; i += 256) {
    int r = i / DIN, k = i % DIN;
    int f = k / D, d = k % D;
    a[r][k] = bf2f(table[(size_t)idx[(r0+r)*F+f]*D + d]);
  }
  __syncthreads();
  float acc[4][2] = {};
  int c0 = threadIdx.x*2;
  for (int k = 0; k < DIN; ++k) {
    unsigned w = *(const unsigned*)(W1 + (size_t)k*H + c0);
    float w0 = __uint_as_float(w<<16), w1 = __uint_as_float(w & 0xFFFF0000u);
    for (int r = 0; r < 4; ++r) { acc[r][0] += a[r][k]*w0; acc[r][1] += a[r][k]*w1; }
  }
  for (int r = 0; r < 4; ++r) {
    float s = 0;
    for (int j = 0; j < 2; ++j) {
      float h = rbf(rbf(acc[r][j]) + bf2f(b1[c0+j]));
      h = h > 0.f ? h : 0.f;
      s += h * bf2f(W2[c0+j]);
    }
    red[r][threadIdx.x] = s;
  }
  __syncthreads();
  if (threadIdx.x < 4) {
    int r = threadIdx.x;
    float s = 0;
    for (int i = 0; i < 256; ++i) s += red[r][i];
    float deep = rbf(rbf(s) + bf2f(b2[0]));
    float w = 0;
    for (int j = 0; j < ND; ++j) w += bf2f(xw[(r0+r)*ND+j]) * bf2f(ww[j]);
    float wide = rbf(rbf(w) + bf2f(bw[0]));
    float z = rbf(wide + deep);
    float p = 1.f/(1.f+__expf(-z));
    out[r0+r] = (u16)(__float_as_uint(rbf(p))>>16);
  }
}

extern "C" void kernel_launch(void* const* d_in, const int* in_sizes, int n_in,
                              void* d_out, int out_size, void* d_ws, size_t ws_size,
                              hipStream_t stream) {
  naive_kernel<<<B/4, 256, 0, stream>>>((const int*)d_in[0], (const u16*)d_in[1], (const u16*)d_in[2], (const u16*)d_in[3],
     (const u16*)d_in[4], (const u16*)d_in[5], (const u16*)d_in[6], (const u16*)d_in[7], (const u16*)d_in[8], (u16*)d_out);
}
```

```cpp
#include <hip/hip_runtime.h>
#include <hip/hip_bf16.h>

__global__ __launch_bounds__(256, 1) void wd_main(const int* idx, const __hip_bfloat16* W1, const __hip_bfloat16* table,
                                                  float* ws, const __hip_bfloat16* b1, const __hip_bfloat16* W2,
                                                  const __hip_bfloat16* xw, const __hip_bfloat16* ww,
                                                  const __hip_bfloat16* bw, const __hip_bfloat16* b2,
                                                  __hip_bfloat16* out) {
  __shared__ __attribute__((aligned(16))) char lds[163840];
  unsigned lbase = (unsigned)(size_t)lds;
  asm volatile(
    "s_load_dwordx2 s[40:41], %0, 0\n"
    "s_load_dwordx2 s[44:45], %0, 8\n"
    "s_load_dwordx16 s[16:31], %0, 16\n"
    "s_load_dwordx2 s[32:33], %0, 80\n"
    "v_and_b32 v1, 63, %2\n"
    "v_lshrrev_b32 v68, 6, %2\n"
    "s_and_b32 s59, %1, 3\n"
    "v_readfirstlane_b32 s56, v68\n"
    "s_bfe_u32 s38, %1, 0x10002\n"
    "s_lshr_b32 s60, %1, 5\n"
    "s_bfe_u32 s58, %1, 0x20003\n"
    "s_lshl3_add_u32 s38, s38, s60\n"
    "s_mov_b32 s61, s59\n"
    "v_and_b32 v71, 15, v1\n"
    "s_movk_i32 s101, 104\n"
    "v_lshrrev_b32 v70, 4, v1\n"
    "s_lshl2_add_u32 s62, s60, s56\n"
    "s_lshl_b32 s62, s62, 11\n"
    "v_and_b32 v68, 2, v1\n"
    "v_lshl_add_u32 v56, v68, 9, s62\n"
    "v_and_b32 v68, 5, v1\n"
    "v_lshl_add_u32 v56, v68, 7, v56\n"
    "s_sub_u32 s62, s60, 6\n"
    "s_min_u32 s60, s60, s62\n"
    "s_lshr_b32 s62, s59, 1\n"
    "s_mul_i32 s62, s62, 13\n"
    "s_and_b32 s96, s59, 1\n"
    "s_mul_i32 s63, s96, 7\n"
    "s_add_u32 s63, s63, s62\n"
    "s_add_u32 s62, s62, 6\n"
    "v_and_b32 v68, 7, v1\n"
    "v_lshrrev_b32 v69, 2, v68\n"
    "v_lshrrev_b32 v69, v69, s58\n"
    "v_xad_u32 v69, v69, v68, s60\n"
    "v_cmp_gt_u32 vcc, 6, v68\n"
    "v_subrev_u32 v72, 6, v69\n"
    "v_min_u32 v69, v69, v72\n"
    "v_add_u32 v69, s63, v69\n"
    "v_mov_b32 v72, s62\n"
    "v_cndmask_b32 v69, v72, v69, vcc\n"
    "s_brev_b32 s15, s58\n"
    "s_lshr_b32 s15, s15, 26\n"
    "v_and_b32 v73, 8, v1\n"
    "v_lshl_add_u32 v72, v73, 1, v70\n"
    "v_xor_b32 v72, s15, v72\n"
    "v_lshlrev_b32 v73, 2, v69\n"
    "v_mad_u32_u24 v6, v72, s101, v73\n"
    "v_xor_b32 v72, 32, v72\n"
    "v_mad_u32_u24 v7, v72, s101, v73\n"
    "s_lshl_b32 s62, s96, 16\n"
    "v_mov_b32 v72, s62\n"
    "v_cndmask_b32 v72, v72, 0, vcc\n"
    "v_lshl_add_u32 v8, v69, 17, v72\n"
    "s_lshl_b32 s62, s38, 6\n"
    "s_add_u32 s62, s62, s56\n"
    "s_mul_i32 s62, s62, 416\n"
    "s_xor_b32 s63, s61, 1\n"
    "s_mul_i32 s34, s63, 6656\n"
    "s_add_u32 s34, s34, s62\n"
    "s_xor_b32 s63, s61, 2\n"
    "s_mul_i32 s35, s63, 6656\n"
    "s_add_u32 s35, s35, s62\n"
    "s_xor_b32 s63, s61, 3\n"
    "s_mul_i32 s36, s63, 6656\n"
    "s_add_u32 s36, s36, s62\n"
    "s_mul_i32 s37, s61, 6656\n"
    "s_add_u32 s37, s37, s62\n"
    "s_mov_b32 s42, 425984\n"
    "s_mov_b32 s43, 0x00020000\n"
    "s_mov_b32 s46, 3407872\n"
    "s_mov_b32 s47, 0x00020000\n"
    "s_lshl_b32 s57, s56, 11\n"
    "v_lshl_or_b32 v8, s58, 8, v8\n"
    "v_lshl_add_u32 v69, s56, 2, v70\n"
    "v_xor_b32 v72, v69, v71\n"
    "v_lshlrev_b32 v3, 4, v72\n"
    "v_add_u32 v2, 1024, v3\n"
    "v_lshrrev_b32 v69, 1, v69\n"
    "v_xor_b32 v69, v69, v68\n"
    "s_lshl_b32 s62, s96, 7\n"
    "v_lshl_add_u32 v5, v69, 4, s62\n"
    "v_add_u32 v4, 1024, v5\n"
    "v_lshrrev_b32 v72, 3, v1\n"
    "v_and_b32 v69, 2, v72\n"
    "v_xor_b32 v69, v69, v68\n"
    "v_lshlrev_b32 v69, 4, v69\n"
    "v_lshl_add_u32 v69, v72, 10, v69\n"
    "s_and_b32 s62, s56, 1\n"
    "s_lshl_b32 s62, s62, 14\n"
    "s_lshr_b32 s63, s56, 1\n"
    "s_lshl_b32 s63, s63, 7\n"
    "s_add_u32 s62, s62, s63\n"
    "v_add_u32 v51, s62, v69\n"
    "v_xor_b32 v69, 64, v51\n"
    "v_add_u32 v52, 7168, v69\n"
    "s_lshl_b32 s39, %1, 8\n"
    "s_add_u32 s39, s39, 12582912\n"
    "v_lshlrev_b32 v55, 2, v1\n"
    "v_mov_b32 v58, 0\n"
    "s_mov_b32 s50, 25601024\n"
    "s_mov_b32 s51, 0x00020000\n"
    "s_mov_b32 s54, 12648448\n"
    "s_mov_b32 s55, 0x00020000\n"
    "s_and_b32 s62, %1, 208\n"
    "s_bcnt1_i32_b32 s62, s62\n"
    "s_bitcmp0_b32 s62, 0\n"
    "s_cbranch_scc1 .Lpx_%=\n"
    "s_waitcnt lgkmcnt(0)\n"
    "s_add_u32 m0, s57, 0\n"
    "v_readlane_b32 s96, v8, 0\n"
    "s_xor_b32 s96, s96, 0\n"
    "buffer_load_dwordx4 v51, s[44:47], s96 offen offset:0 lds\n"
    "buffer_load_dwordx4 v52, s[44:47], s96 offen offset:1024 lds\n"
    "s_sub_u32 s48, s16, 1024\n"
    "s_subb_u32 s49, s17, 0\n"
    "s_mov_b64 s[52:53], s[18:19]\n"
    "s_add_u32 m0, s57, 8192\n"
    "s_xor_b32 s96, s96, 32768\n"
    "buffer_load_dwordx4 v51, s[44:47], s96 offen offset:0 lds\n"
    "buffer_load_dwordx4 v52, s[44:47], s96 offen offset:1024 lds\n"
    "s_add_u32 m0, s57, 16384\n"
    "s_xor_b32 s96, s96, 98304\n"
    "buffer_load_dwordx4 v51, s[44:47], s96 offen offset:0 lds\n"
    "buffer_load_dwordx4 v52, s[44:47], s96 offen offset:1024 lds\n"
    "s_add_u32 m0, s57, 24576\n"
    "s_xor_b32 s96, s96, 32768\n"
    "buffer_load_dwordx4 v51, s[44:47], s96 offen offset:0 lds\n"
    "buffer_load_dwordx4 v52, s[44:47], s96 offen offset:1024 lds\n"
    "buffer_store_dword v58, v55, s[52:55], s39 offen sc1\n"
    "s_getpc_b64 s[98:99]\n"
    ".Lpc_b%=:\n"
    "s_lshr_b32 s62, %1, 5\n"
    "s_lshl_b32 s62, s62, 1\n"
    "s_bfe_u32 s63, %1, 65539\n"
    "s_or_b32 s62, s62, s63\n"
    "s_lshl2_add_u32 s62, s62, s56\n"
    "s_mul_i32 s62, s62, 384\n"
    "s_min_u32 s62, s62, .Lend_b%=-.Lbody_b%=-328\n"
    "s_add_u32 s62, s62, .Lbody_b%=-.Lpc_b%=\n"
    "s_load_dword s100, s[98:99], s62 offset:0\n"
    "s_load_dword s100, s[98:99], s62 offset:64\n"
    "s_load_dword s100, s[98:99], s62 offset:128\n"
    "s_load_dword s100, s[98:99], s62 offset:192\n"
    "s_load_dword s100, s[98:99], s62 offset:256\n"
    "s_load_dword s100, s[98:99], s62 offset:320\n"
    "s_branch .Lalt_%=\n"
    ".Lpx_%=:\n"
    "s_waitcnt lgkmcnt(0)\n"
    "buffer_load_dword v9, v6, s[40:43], s34 offen\n"
    "s_sub_u32 s48, s16, 1024\n"
    "s_subb_u32 s49, s17, 0\n"
    "s_mov_b64 s[52:53], s[18:19]\n"
    "buffer_load_dword v10, v7, s[40:43], s34 offen\n"
    "buffer_load_dword v11, v6, s[40:43], s35 offen\n"
    "buffer_load_dword v12, v7, s[40:43], s35 offen\n"
    "buffer_load_dword v13, v6, s[40:43], s36 offen\n"
    "s_add_u32 m0, s57, 0\n"
    "v_readlane_b32 s96, v8, 0\n"
    "s_xor_b32 s96, s96, 0\n"
    "buffer_load_dwordx4 v51, s[44:47], s96 offen offset:0 lds\n"
    "buffer_load_dwordx4 v52, s[44:47], s96 offen offset:1024 lds\n"
    "buffer_load_dword v14, v7, s[40:43], s36 offen\n"
    "buffer_store_dword v58, v55, s[52:55], s39 offen sc1\n"
    "s_getpc_b64 s[98:99]\n"
    ".Lpc_%=:\n"
    "s_lshr_b32 s62, %1, 5\n"
    "s_lshl_b32 s62, s62, 1\n"
    "s_bfe_u32 s63, %1, 65539\n"
    "s_or_b32 s62, s62, s63\n"
    "s_lshl2_add_u32 s62, s62, s56\n"
    "s_mul_i32 s62, s62, 384\n"
    "s_min_u32 s62, s62, .Lend_%=-.Lbody_%=-328\n"
    "s_add_u32 s62, s62, .Lbody_%=-.Lpc_%=\n"
    "s_load_dword s100, s[98:99], s62 offset:0\n"
    "s_load_dword s100, s[98:99], s62 offset:64\n"
    "s_load_dword s100, s[98:99], s62 offset:128\n"
    "s_load_dword s100, s[98:99], s62 offset:192\n"
    "s_load_dword s100, s[98:99], s62 offset:256\n"
    "s_load_dword s100, s[98:99], s62 offset:320\n"
    ".Lbody_%=:\n"
    "s_lshl_b32 s63, s56, 12\n"
    "v_lshl_add_u32 v53, v1, 4, s63\n"
    "s_mul_i32 s98, %1, 49152\n"
    "v_and_b32 v72, 1, v1\n"
    "v_lshl_add_u32 v72, s56, 1, v72\n"
    "s_cmp_lt_u32 s56, 2\n"
    "s_cselect_b32 s63, 1, 8\n"
    "v_and_b32 v73, 3, v72\n"
    "v_mul_u32_u24 v73, s63, v73\n"
    "v_lshlrev_b32 v73, 8, v73\n"
    "v_xor_b32 v73, s39, v73\n"
    "v_lshl_add_u32 v54, v72, 2, v73\n"
    "v_add_u32 v54, 128, v54\n"
    "v_mov_b32 v57, 1\n"
    "s_lshl2_add_u32 s62, s58, s56\n"
    "s_lshl_b32 s63, s62, 6\n"
    "v_lshl_add_u32 v65, v70, 3, s63\n"
    "s_lshl2_add_u32 s99, s38, s61\n"
    "s_lshl_b32 s99, s99, 6\n"
    "v_xor_b32 v64, s15, v1\n"
    "v_add_u32 v64, s99, v64\n"
    "v_mul_u32_u24 v64, 26, v64\n"
    "s_min_u32 s63, s62, 12\n"
    "s_lshl_b32 s63, s63, 1\n"
    "v_add_u32 v64, s63, v64\n"
    "s_add_u32 s26, s26, s63\n"
    "s_addc_u32 s27, s27, 0\n"
    "s_cmp_eq_u32 s62, 13\n"
    "s_cselect_b64 s[26:27], s[28:29], s[26:27]\n"
    "s_cmp_eq_u32 s62, 14\n"
    "s_cselect_b64 s[26:27], s[30:31], s[26:27]\n"
    "s_cmp_lt_u32 s62, 13\n"
    "s_cselect_b32 s16, -1, 0\n"
    "s_cselect_b32 s17, 0, 1.0\n"
    "s_cmp_lt_u32 s62, 15\n"
    "s_cselect_b32 s60, -1, 0\n"
    "v_lshrrev_b32 v68, 2, v71\n"
    "v_lshlrev_b32 v69, 11, v68\n"
    "v_and_b32 v73, 3, v1\n"
    "v_lshl_add_u32 v69, v73, 8, v69\n"
    "v_xor_b32 v72, v70, v71\n"
    "v_lshl_add_u32 v21, v72, 4, v69\n"
    "v_xor_b32 v22, 64, v21\n"
    "v_xor_b32 v23, 128, v21\n"
    "v_xor_b32 v24, 192, v21\n"
    "v_lshrrev_b32 v72, 1, v71\n"
    "v_xor_b32 v72, v70, v72\n"
    "v_lshl_add_u32 v69, v72, 4, v69\n"
    "v_and_b32 v72, 1, v1\n"
    "v_lshl_add_u32 v33, v72, 7, v69\n"
    "v_xor_b32 v34, 64, v33\n"
    "v_xor_b32 v35, 128, v33\n"
    "v_xor_b32 v36, 192, v33\n"
    "s_lshr_b32 s63, s56, 1\n"
    "s_lshl_b32 s63, s63, 12\n"
    "v_lshl_add_u32 v69, v70, 3, v68\n"
    "v_lshl_add_u32 v69, v69, 7, s63\n"
    "v_lshl_add_u32 v69, v73, 3, v69\n"
    "v_bfe_u32 v72, v1, 3, 2\n"
    "s_and_b32 s63, s56, 1\n"
    "s_lshl_b32 s63, s63, 1\n"
    "v_xor_b32 v72, s63, v72\n"
    "v_lshl_add_u32 v45, v72, 5, v69\n"
    "v_xor_b32 v46, 32, v45\n"
    "v_add_u32 v25, 65536, v21\n"
    "v_add_u32 v26, 65536, v22\n"
    "v_add_u32 v27, 65536, v23\n"
    "v_add_u32 v28, 65536, v24\n"
    "v_add_u32 v37, 65536, v33\n"
    "v_add_u32 v38, 65536, v34\n"
    "v_add_u32 v39, 65536, v35\n"
    "v_add_u32 v40, 65536, v36\n"
    "v_add_u32 v47, 65536, v45\n"
    "v_add_u32 v48, 65536, v46\n"
    "v_add_u32 v29, 131072, v21\n"
    "v_add_u32 v30, 131072, v22\n"
    "v_add_u32 v31, 131072, v23\n"
    "v_add_u32 v32, 131072, v24\n"
    "v_add_u32 v41, 131072, v33\n"
    "v_add_u32 v42, 131072, v34\n"
    "v_add_u32 v43, 131072, v35\n"
    "v_add_u32 v44, 131072, v36\n"
    "v_add_u32 v49, 131072, v45\n"
    "v_add_u32 v50, 131072, v46\n"
    "v_cmp_eq_u32 vcc, v68, v70\n"
    "v_mov_b32 v74, 0x3f80\n"
    "v_mov_b32 v75, 0\n"
    "v_lshlrev_b32 v73, 4, v73\n"
    "v_cndmask_b32 v74, 0, v74, vcc\n"
    "v_mov_b32 v92, 0\n"
    "v_mov_b32 v93, 0\n"
    "v_mov_b32 v96, 0\n"
    "v_mov_b32 v97, 0\n"
    "v_lshlrev_b64 v[94:95], v73, v[74:75]\n"
    "s_lshl2_add_u32 s59, s56, s39\n"
    "s_add_u32 m0, s57, 8192\n"
    "s_xor_b32 s96, s96, 65536\n"
    "buffer_load_dwordx4 v51, s[44:47], s96 offen offset:0 lds\n"
    "buffer_load_dwordx4 v52, s[44:47], s96 offen offset:1024 lds\n"
    "s_add_u32 m0, s57, 16384\n"
    "s_xor_b32 s96, s96, 98304\n"
    "buffer_load_dwordx4 v51, s[44:47], s96 offen offset:0 lds\n"
    "buffer_load_dwordx4 v52, s[44:47], s96 offen offset:1024 lds\n"
    "s_add_u32 m0, s57, 24576\n"
    "s_xor_b32 s96, s96, 65536\n"
    "buffer_load_dwordx4 v51, s[44:47], s96 offen offset:0 lds\n"
    "buffer_load_dwordx4 v52, s[44:47], s96 offen offset:1024 lds\n"
    "v_readlane_b32 s62, v8, 6\n"
    "s_andn2_b32 s62, s62, 512\n"
    "buffer_load_dword v59, v56, s[44:47], s62 offen\n"
    "s_waitcnt vmcnt(9)\n"
    "s_barrier\n"
    "v_lshlrev_b32 v9, 8, v9\n"
    "v_lshlrev_b32 v10, 8, v10\n"
    "v_lshlrev_b32 v11, 8, v11\n"
    "v_lshlrev_b32 v12, 8, v12\n"
    "v_lshlrev_b32 v13, 8, v13\n"
    "s_add_u32 m0, s57, 32768\n"
    "v_add_u32_dpp v17, v9, v2 row_newbcast:0 row_mask:0xf bank_mask:0xf\n"
    "buffer_load_dwordx4 v17, s[48:51], 0 offen offset:0 lds\n"
    "ds_read_b64_tr_b16 a[0:1], v45 offset:0\n"
    "ds_read_b64_tr_b16 a[2:3], v45 offset:512\n"
    "ds_read_b64_tr_b16 a[4:5], v46 offset:0\n"
    "ds_read_b64_tr_b16 a[6:7], v46 offset:512\n"
    "v_add_u32_dpp v18, v9, v3 row_newbcast:8 row_mask:0xf bank_mask:0xf\n"
    "buffer_load_dwordx4 v18, s[48:51], 0 offen offset:1024 lds\n"
    "s_add_u32 m0, s57, 40960\n"
    "v_add_u32_dpp v19, v10, v2 row_newbcast:0 row_mask:0xf bank_mask:0xf\n"
    "buffer_load_dwordx4 v19, s[48:51], 0 offen offset:0 lds\n"
    "v_add_u32_dpp v20, v10, v3 row_newbcast:8 row_mask:0xf bank_mask:0xf\n"
    "buffer_load_dwordx4 v20, s[48:51], 0 offen offset:1024 lds\n"
    "s_add_u32 m0, s57, 49152\n"
    "v_add_u32_dpp v17, v11, v2 row_newbcast:0 row_mask:0xf bank_mask:0xf\n"
    "buffer_load_dwordx4 v17, s[48:51], 0 offen offset:0 lds\n"
    "v_add_u32_dpp v18, v11, v3 row_newbcast:8 row_mask:0xf bank_mask:0xf\n"
    "buffer_load_dwordx4 v18, s[48:51], 0 offen offset:1024 lds\n"
    "s_add_u32 m0, s57, 57344\n"
    "v_add_u32_dpp v19, v12, v2 row_newbcast:0 row_mask:0xf bank_mask:0xf\n"
    "buffer_load_dwordx4 v19, s[48:51], 0 offen offset:0 lds\n"
    "s_waitcnt vmcnt(8) lgkmcnt(0)\n"
    "s_barrier\n"
    "v_lshlrev_b32 v14, 8, v14\n"
    "ds_read_b64_tr_b16 a[8:9], v45 offset:16384\n"
    "ds_read_b64_tr_b16 a[10:11], v45 offset:16896\n"
    "ds_read_b64_tr_b16 a[16:17], v45 offset:8192\n"
    "ds_read_b64_tr_b16 a[18:19], v45 offset:8704\n"
    "ds_read_b64_tr_b16 a[24:25], v45 offset:24576\n"
    "ds_read_b64_tr_b16 a[26:27], v45 offset:25088\n"
    "ds_read_b64_tr_b16 a[12:13], v46 offset:16384\n"
    "ds_read_b64_tr_b16 a[14:15], v46 offset:16896\n"
    "ds_read_b64_tr_b16 a[20:21], v46 offset:8192\n"
    "ds_read_b64_tr_b16 a[22:23], v46 offset:8704\n"
    "ds_read_b64_tr_b16 a[28:29], v46 offset:24576\n"
    "ds_read_b64_tr_b16 a[30:31], v46 offset:25088\n"
    "v_add_u32_dpp v20, v12, v3 row_newbcast:8 row_mask:0xf bank_mask:0xf\n"
    "buffer_load_dwordx4 v20, s[48:51], 0 offen offset:1024 lds\n"
    "s_add_u32 m0, s57, 65536\n"
    "v_readlane_b32 s96, v8, 5\n"
    "s_xor_b32 s96, s96, 0\n"
    "buffer_load_dwordx4 v51, s[44:47], s96 offen offset:0 lds\n"
    "buffer_load_dwordx4 v52, s[44:47], s96 offen offset:1024 lds\n"
    "s_add_u32 m0, s57, 73728\n"
    "v_add_u32_dpp v17, v13, v2 row_newbcast:0 row_mask:0xf bank_mask:0xf\n"
    "buffer_load_dwordx4 v17, s[48:51], 0 offen offset:0 lds\n"
    "v_add_u32_dpp v18, v13, v3 row_newbcast:8 row_mask:0xf bank_mask:0xf\n"
    "buffer_load_dwordx4 v18, s[48:51], 0 offen offset:1024 lds\n"
    "s_waitcnt vmcnt(8) lgkmcnt(0)\n"
    "s_barrier\n"
    "ds_read_b128 a[208:211], v21 offset:32768\n"
    "ds_read_b128 a[212:215], v22 offset:32768\n"
    "s_waitcnt lgkmcnt(1)\n"
    "v_mfma_f32_16x16x32_bf16 v[104:107], a[0:3], a[208:211], 0\n"
    "s_waitcnt lgkmcnt(0)\n"
    "ds_read_b128 a[216:219], v23 offset:32768\n"
    "ds_read_b128 a[220:223], v24 offset:32768\n"
    "v_mfma_f32_16x16x32_bf16 v[104:107], a[8:11], a[212:215], v[104:107]\n"
    "s_waitcnt lgkmcnt(1)\n"
    "ds_read_b128 a[224:227], v21 offset:33792\n"
    "ds_read_b128 a[228:231], v22 offset:33792\n"
    "v_mfma_f32_16x16x32_bf16 v[104:107], a[16:19], a[216:219], v[104:107]\n"
    "s_waitcnt lgkmcnt(2)\n"
    "ds_read_b128 a[232:235], v23 offset:33792\n"
    "v_mfma_f32_16x16x32_bf16 v[104:107], a[24:27], a[220:223], v[104:107]\n"
    "ds_read_b128 a[236:239], v24 offset:33792\n"
    "v_mfma_f32_16x16x32_bf16 v[108:111], a[28:31], a[220:223], 0\n"
    "s_add_u32 m0, s57, 81920\n"
    "ds_read_b128 a[240:243], v21 offset:40960\n"
    "v_add_u32_dpp v19, v14, v2 row_newbcast:0 row_mask:0xf bank_mask:0xf\n"
    "v_mfma_f32_16x16x32_bf16 v[108:111], a[20:23], a[216:219], v[108:111]\n"
    "ds_read_b128 a[244:247], v22 offset:40960\n"
    "v_mfma_f32_16x16x32_bf16 v[108:111], a[12:15], a[212:215], v[108:111]\n"
    "ds_read_b128 a[248:251], v23 offset:40960\n"
    "v_mfma_f32_16x16x32_bf16 v[108:111], a[4:7], a[208:211], v[108:111]\n"
    "s_waitcnt lgkmcnt(6)\n"
    "ds_read_b128 a[252:255], v24 offset:40960\n"
    "buffer_load_dwordx4 v19, s[48:51], 0 offen offset:0 lds\n"
    "v_mfma_f32_16x16x32_bf16 v[116:119], a[4:7], a[224:227], 0\n"
    "s_waitcnt lgkmcnt(6)\n"
    "ds_read_b128 v[232:235], v21 offset:41984\n"
    "v_add_u32_dpp v20, v14, v3 row_newbcast:8 row_mask:0xf bank_mask:0xf\n"
    "v_mfma_f32_16x16x32_bf16 v[116:119], a[12:15], a[228:231], v[116:119]\n"
    "s_waitcnt lgkmcnt(6)\n"
    "ds_read_b128 v[236:239], v22 offset:41984\n"
    "v_mfma_f32_16x16x32_bf16 v[116:119], a[20:23], a[232:235], v[116:119]\n"
    "s_waitcnt lgkmcnt(6)\n"
    "ds_read_b128 v[240:243], v23 offset:41984\n"
    "v_mfma_f32_16x16x32_bf16 v[116:119], a[28:31], a[236:239], v[116:119]\n"
    "ds_read_b128 v[244:247], v24 offset:41984\n"
    "v_mfma_f32_16x16x32_bf16 v[112:115], a[24:27], a[236:239], 0\n"
    "v_mfma_f32_16x16x32_bf16 v[112:115], a[16:19], a[232:235], v[112:115]\n"
    "v_mfma_f32_16x16x32_bf16 v[112:115], a[8:11], a[228:231], v[112:115]\n"
    "v_mfma_f32_16x16x32_bf16 v[112:115], a[0:3], a[224:227], v[112:115]\n"
    "buffer_load_dwordx4 v20, s[48:51], 0 offen offset:1024 lds\n"
    "buffer_store_dword v57, v54, s[52:55], 0 offen sc1\n"
    "s_add_u32 m0, s57, 90112\n"
    "v_readlane_b32 s96, v8, 1\n"
    "s_xor_b32 s96, s96, 0\n"
    "buffer_load_dwordx4 v51, s[44:47], s96 offen offset:0 lds\n"
    "buffer_load_dwordx4 v52, s[44:47], s96 offen offset:1024 lds\n"
    "s_waitcnt vmcnt(9) lgkmcnt(0)\n"
    "s_barrier\n"
    "v_mfma_f32_16x16x32_bf16 v[120:123], a[0:3], a[240:243], 0\n"
    "s_add_u32 m0, s57, 98304\n"
    "ds_read_b128 a[208:211], v21 offset:49152\n"
    "s_xor_b32 s96, s96, 32768\n"
    "v_mfma_f32_16x16x32_bf16 v[120:123], a[8:11], a[244:247], v[120:123]\n"
    "ds_read_b128 a[212:215], v22 offset:49152\n"
    "buffer_load_dwordx4 v51, s[44:47], s96 offen offset:0 lds\n"
    "v_mfma_f32_16x16x32_bf16 v[120:123], a[16:19], a[248:251], v[120:123]\n"
    "ds_read_b128 a[216:219], v23 offset:49152\n"
    "v_mfma_f32_16x16x32_bf16 v[120:123], a[24:27], a[252:255], v[120:123]\n"
    "ds_read_b128 a[220:223], v24 offset:49152\n"
    "v_mfma_f32_16x16x32_bf16 v[124:127], a[28:31], a[252:255], 0\n"
    "ds_read_b128 a[224:227], v21 offset:50176\n"
    "v_mfma_f32_16x16x32_bf16 v[124:127], a[20:23], a[248:251], v[124:127]\n"
    "ds_read_b128 a[228:231], v22 offset:50176\n"
    "buffer_load_dwordx4 v52, s[44:47], s96 offen offset:1024 lds\n"
    "v_mfma_f32_16x16x32_bf16 v[124:127], a[12:15], a[244:247], v[124:127]\n"
    "ds_read_b128 a[232:235], v23 offset:50176\n"
    "v_mfma_f32_16x16x32_bf16 v[124:127], a[4:7], a[240:243], v[124:127]\n"
    "ds_read_b128 a[236:239], v24 offset:50176\n"
    "v_mfma_f32_16x16x32_bf16 v[132:135], a[4:7], v[232:235], 0\n"
    "ds_read_b128 a[240:243], v21 offset:57344\n"
    "v_mfma_f32_16x16x32_bf16 v[132:135], a[12:15], v[236:239], v[132:135]\n"
    "ds_read_b128 a[244:247], v22 offset:57344\n"
    "v_mfma_f32_16x16x32_bf16 v[132:135], a[20:23], v[240:243], v[132:135]\n"
    "ds_read_b128 a[248:251], v23 offset:57344\n"
    "v_mfma_f32_16x16x32_bf16 v[132:135], a[28:31], v[244:247], v[132:135]\n"
    "ds_read_b128 a[252:255], v24 offset:57344\n"
    "v_mfma_f32_16x16x32_bf16 v[128:131], a[24:27], v[244:247], 0\n"
    "v_mfma_f32_16x16x32_bf16 v[128:131], a[16:19], v[240:243], v[128:131]\n"
    "v_mfma_f32_16x16x32_bf16 v[128:131], a[8:11], v[236:239], v[128:131]\n"
    "v_mfma_f32_16x16x32_bf16 v[128:131], a[0:3], v[232:235], v[128:131]\n"
    "s_waitcnt lgkmcnt(3)\n"
    "ds_read_b128 v[232:235], v21 offset:58368\n"
    "v_mfma_f32_16x16x32_bf16 v[136:139], a[0:3], a[208:211], 0\n"
    "ds_read_b128 v[236:239], v22 offset:58368\n"
    "v_mfma_f32_16x16x32_bf16 v[136:139], a[8:11], a[212:215], v[136:139]\n"
    "ds_read_b128 v[240:243], v23 offset:58368\n"
    "v_mfma_f32_16x16x32_bf16 v[136:139], a[16:19], a[216:219], v[136:139]\n"
    "ds_read_b128 v[244:247], v24 offset:58368\n"
    "v_mfma_f32_16x16x32_bf16 v[136:139], a[24:27], a[220:223], v[136:139]\n"
    "v_mfma_f32_16x16x32_bf16 v[140:143], a[28:31], a[220:223], 0\n"
    "v_mfma_f32_16x16x32_bf16 v[140:143], a[20:23], a[216:219], v[140:143]\n"
    "v_mfma_f32_16x16x32_bf16 v[140:143], a[12:15], a[212:215], v[140:143]\n"
    "v_mfma_f32_16x16x32_bf16 v[140:143], a[4:7], a[208:211], v[140:143]\n"
    "s_add_u32 m0, s57, 106496\n"
    "s_xor_b32 s96, s96, 98304\n"
    "v_mfma_f32_16x16x32_bf16 v[148:151], a[4:7], a[224:227], 0\n"
    "v_mfma_f32_16x16x32_bf16 v[148:151], a[12:15], a[228:231], v[148:151]\n"
    "v_mfma_f32_16x16x32_bf16 v[148:151], a[20:23], a[232:235], v[148:151]\n"
    "buffer_load_dwordx4 v51, s[44:47], s96 offen offset:0 lds\n"
    "v_mfma_f32_16x16x32_bf16 v[148:151], a[28:31], a[236:239], v[148:151]\n"
    "v_mfma_f32_16x16x32_bf16 v[144:147], a[24:27], a[236:239], 0\n"
    "v_mfma_f32_16x16x32_bf16 v[144:147], a[16:19], a[232:235], v[144:147]\n"
    "v_mfma_f32_16x16x32_bf16 v[144:147], a[8:11], a[228:231], v[144:147]\n"
    "v_mfma_f32_16x16x32_bf16 v[144:147], a[0:3], a[224:227], v[144:147]\n"
    "buffer_load_dwordx4 v52, s[44:47], s96 offen offset:1024 lds\n"
    "s_add_u32 m0, s57, 114688\n"
    "s_xor_b32 s96, s96, 32768\n"
    "buffer_load_dwordx4 v51, s[44:47], s96 offen offset:0 lds\n"
    "buffer_load_dwordx4 v52, s[44:47], s96 offen offset:1024 lds\n"
    "s_waitcnt vmcnt(9) lgkmcnt(0)\n"
    "s_barrier\n"
    "v_mfma_f32_16x16x32_bf16 v[152:155], a[0:3], a[240:243], 0\n"
    "s_add_u32 m0, s57, 122880\n"
    "ds_read_b128 a[208:211], v25 offset:8192\n"
    "v_add_u32_dpp v17, v10, v2 row_newbcast:1 row_mask:0xf bank_mask:0xf\n"
    "v_mfma_f32_16x16x32_bf16 v[152:155], a[8:11], a[244:247], v[152:155]\n"
    "ds_read_b128 a[212:215], v26 offset:8192\n"
    "buffer_load_dwordx4 v17, s[48:51], 0 offen offset:0 lds\n"
    "v_mfma_f32_16x16x32_bf16 v[152:155], a[16:19], a[248:251], v[152:155]\n"
    "ds_read_b128 a[216:219], v27 offset:8192\n"
    "v_add_u32_dpp v18, v10, v3 row_newbcast:9 row_mask:0xf bank_mask:0xf\n"
    "v_mfma_f32_16x16x32_bf16 v[152:155], a[24:27], a[252:255], v[152:155]\n"
    "ds_read_b128 a[220:223], v28 offset:8192\n"
    "v_mfma_f32_16x16x32_bf16 v[156:159], a[28:31], a[252:255], 0\n"
    "ds_read_b128 a[224:227], v25 offset:9216\n"
    "v_mfma_f32_16x16x32_bf16 v[156:159], a[20:23], a[248:251], v[156:159]\n"
    "ds_read_b128 a[228:231], v26 offset:9216\n"
    "buffer_load_dwordx4 v18, s[48:51], 0 offen offset:1024 lds\n"
    "v_mfma_f32_16x16x32_bf16 v[156:159], a[12:15], a[244:247], v[156:159]\n"
    "s_add_u32 m0, s57, 131072\n"
    "ds_read_b128 a[232:235], v27 offset:9216\n"
    "v_add_u32_dpp v19, v9, v2 row_newbcast:1 row_mask:0xf bank_mask:0xf\n"
    "v_mfma_f32_16x16x32_bf16 v[156:159], a[4:7], a[240:243], v[156:159]\n"
    "ds_read_b128 a[236:239], v28 offset:9216\n"
    "v_mfma_f32_16x16x32_bf16 v[164:167], a[4:7], v[232:235], 0\n"
    "ds_read_b128 a[240:243], v25 offset:16384\n"
    "v_mfma_f32_16x16x32_bf16 v[164:167], a[12:15], v[236:239], v[164:167]\n"
    "ds_read_b128 a[244:247], v26 offset:16384\n"
    "buffer_load_dwordx4 v19, s[48:51], 0 offen offset:0 lds\n"
    "v_mfma_f32_16x16x32_bf16 v[164:167], a[20:23], v[240:243], v[164:167]\n"
    "ds_read_b128 a[248:251], v27 offset:16384\n"
    "v_add_u32_dpp v20, v9, v3 row_newbcast:9 row_mask:0xf bank_mask:0xf\n"
    "v_mfma_f32_16x16x32_bf16 v[164:167], a[28:31], v[244:247], v[164:167]\n"
    "ds_read_b128 a[252:255], v28 offset:16384\n"
    "v_mfma_f32_16x16x32_bf16 v[160:163], a[24:27], v[244:247], 0\n"
    "ds_read_b64_tr_b16 a[160:161], v47 offset:0\n"
    "v_mfma_f32_16x16x32_bf16 v[160:163], a[16:19], v[240:243], v[160:163]\n"
    "ds_read_b64_tr_b16 a[162:163], v47 offset:512\n"
    "buffer_load_dwordx4 v20, s[48:51], 0 offen offset:1024 lds\n"
    "v_mfma_f32_16x16x32_bf16 v[160:163], a[8:11], v[236:239], v[160:163]\n"
    "s_add_u32 m0, s57, 139264\n"
    "ds_read_b64_tr_b16 a[164:165], v48 offset:0\n"
    "v_add_u32_dpp v17, v12, v2 row_newbcast:1 row_mask:0xf bank_mask:0xf\n"
    "v_mfma_f32_16x16x32_bf16 v[160:163], a[0:3], v[232:235], v[160:163]\n"
    "s_waitcnt lgkmcnt(6)\n"
    "ds_read_b128 v[232:235], v25 offset:17408\n"
    "v_mfma_f32_16x16x32_bf16 v[168:171], a[0:3], a[208:211], 0\n"
    "ds_read_b128 v[236:239], v26 offset:17408\n"
    "v_mfma_f32_16x16x32_bf16 v[168:171], a[8:11], a[212:215], v[168:171]\n"
    "ds_read_b128 v[240:243], v27 offset:17408\n"
    "buffer_load_dwordx4 v17, s[48:51], 0 offen offset:0 lds\n"
    "v_mfma_f32_16x16x32_bf16 v[168:171], a[16:19], a[216:219], v[168:171]\n"
    "ds_read_b128 v[244:247], v28 offset:17408\n"
    "v_add_u32_dpp v18, v12, v3 row_newbcast:9 row_mask:0xf bank_mask:0xf\n"
    "v_mfma_f32_16x16x32_bf16 v[168:171], a[24:27], a[220:223], v[168:171]\n"
    "ds_read_b64_tr_b16 a[166:167], v48 offset:512\n"
    "v_mfma_f32_16x16x32_bf16 v[172:175], a[28:31], a[220:223], 0\n"
    "v_mfma_f32_16x16x32_bf16 v[172:175], a[20:23], a[216:219], v[172:175]\n"
    "buffer_load_dwordx4 v18, s[48:51], 0 offen offset:1024 lds\n"
    "v_mfma_f32_16x16x32_bf16 v[172:175], a[12:15], a[212:215], v[172:175]\n"
    "s_add_u32 m0, s57, 147456\n"
    "v_readlane_b32 s96, v8, 3\n"
    "v_mfma_f32_16x16x32_bf16 v[172:175], a[4:7], a[208:211], v[172:175]\n"
    "s_xor_b32 s96, s96, 0\n"
    "v_mfma_f32_16x16x32_bf16 v[180:183], a[4:7], a[224:227], 0\n"
    "v_mfma_f32_16x16x32_bf16 v[180:183], a[12:15], a[228:231], v[180:183]\n"
    "buffer_load_dwordx4 v51, s[44:47], s96 offen offset:0 lds\n"
    "v_mfma_f32_16x16x32_bf16 v[180:183], a[20:23], a[232:235], v[180:183]\n"
    "v_mfma_f32_16x16x32_bf16 v[180:183], a[28:31], a[236:239], v[180:183]\n"
    "v_mfma_f32_16x16x32_bf16 v[176:179], a[24:27], a[236:239], 0\n"
    "v_mfma_f32_16x16x32_bf16 v[176:179], a[16:19], a[232:235], v[176:179]\n"
    "buffer_load_dwordx4 v52, s[44:47], s96 offen offset:1024 lds\n"
    "s_add_u32 m0, s57, 155648\n"
    "v_mfma_f32_16x16x32_bf16 v[176:179], a[8:11], a[228:231], v[176:179]\n"
    "s_xor_b32 s96, s96, 32768\n"
    "v_mfma_f32_16x16x32_bf16 v[176:179], a[0:3], a[224:227], v[176:179]\n"
    "buffer_load_dwordx4 v51, s[44:47], s96 offen offset:0 lds\n"
    "buffer_load_dwordx4 v52, s[44:47], s96 offen offset:1024 lds\n"
    "s_add_u32 m0, s57, 0\n"
    "s_xor_b32 s96, s96, 98304\n"
    "buffer_load_dwordx4 v51, s[44:47], s96 offen offset:0 lds\n"
    "buffer_load_dwordx4 v52, s[44:47], s96 offen offset:1024 lds\n"
    "s_waitcnt vmcnt(8) lgkmcnt(0)\n"
    "s_barrier\n"
    "ds_read_b64_tr_b16 a[32:33], v47 offset:24576\n"
    "ds_read_b64_tr_b16 a[34:35], v47 offset:25088\n"
    "v_mfma_f32_16x16x32_bf16 v[184:187], a[0:3], a[240:243], 0\n"
    "s_add_u32 m0, s57, 16384\n"
    "ds_read_b128 a[208:211], v25 offset:57344\n"
    "ds_read_b64_tr_b16 a[40:41], v47 offset:32768\n"
    "v_mfma_f32_16x16x32_bf16 v[184:187], a[8:11], a[244:247], v[184:187]\n"
    "s_xor_b32 s96, s96, 32768\n"
    "ds_read_b64_tr_b16 a[42:43], v47 offset:33280\n"
    "ds_read_b128 a[212:215], v26 offset:57344\n"
    "v_mfma_f32_16x16x32_bf16 v[184:187], a[16:19], a[248:251], v[184:187]\n"
    "ds_read_b64_tr_b16 a[48:49], v47 offset:40960\n"
    "buffer_load_dwordx4 v51, s[44:47], s96 offen offset:0 lds\n"
    "v_mfma_f32_16x16x32_bf16 v[184:187], a[24:27], a[252:255], v[184:187]\n"
    "ds_read_b64_tr_b16 a[50:51], v47 offset:41472\n"
    "v_mfma_f32_16x16x32_bf16 v[188:191], a[28:31], a[252:255], 0\n"
    "ds_read_b128 a[216:219], v27 offset:57344\n"
    "buffer_load_dwordx4 v52, s[44:47], s96 offen offset:1024 lds\n"
    "v_mfma_f32_16x16x32_bf16 v[188:191], a[20:23], a[248:251], v[188:191]\n"
    "s_add_u32 m0, s57, 8192\n"
    "ds_read_b64_tr_b16 a[56:57], v47 offset:49152\n"
    "v_readlane_b32 s96, v8, 2\n"
    "v_mfma_f32_16x16x32_bf16 v[188:191], a[12:15], a[244:247], v[188:191]\n"
    "s_xor_b32 s96, s96, 0\n"
    "ds_read_b64_tr_b16 a[58:59], v47 offset:49664\n"
    "v_mfma_f32_16x16x32_bf16 v[188:191], a[4:7], a[240:243], v[188:191]\n"
    "ds_read_b128 a[220:223], v28 offset:57344\n"
    "v_mfma_f32_16x16x32_bf16 v[196:199], a[4:7], v[232:235], 0\n"
    "ds_read_b64_tr_b16 a[60:61], v48 offset:49152\n"
    "buffer_load_dwordx4 v51, s[44:47], s96 offen offset:0 lds\n"
    "v_mfma_f32_16x16x32_bf16 v[196:199], a[12:15], v[236:239], v[196:199]\n"
    "ds_read_b64_tr_b16 a[62:63], v48 offset:49664\n"
    "v_mfma_f32_16x16x32_bf16 v[196:199], a[20:23], v[240:243], v[196:199]\n"
    "ds_read_b64_tr_b16 a[52:53], v48 offset:40960\n"
    "v_mfma_f32_16x16x32_bf16 v[196:199], a[28:31], v[244:247], v[196:199]\n"
    "ds_read_b64_tr_b16 a[54:55], v48 offset:41472\n"
    "v_mfma_f32_16x16x32_bf16 v[192:195], a[24:27], v[244:247], 0\n"
    "ds_read_b64_tr_b16 a[44:45], v48 offset:32768\n"
    "buffer_load_dwordx4 v52, s[44:47], s96 offen offset:1024 lds\n"
    "v_mfma_f32_16x16x32_bf16 v[192:195], a[16:19], v[240:243], v[192:195]\n"
    "s_add_u32 m0, s57, 24576\n"
    "ds_read_b64_tr_b16 a[46:47], v48 offset:33280\n"
    "s_xor_b32 s96, s96, 32768\n"
    "v_mfma_f32_16x16x32_bf16 v[192:195], a[8:11], v[236:239], v[192:195]\n"
    "ds_read_b64_tr_b16 a[36:37], v48 offset:24576\n"
    "v_mfma_f32_16x16x32_bf16 v[192:195], a[0:3], v[232:235], v[192:195]\n"
    "s_waitcnt lgkmcnt(6)\n"
    "ds_read_b64_tr_b16 a[38:39], v48 offset:25088\n"
    "v_mfma_f32_16x16x32_bf16 v[120:123], a[32:35], a[208:211], v[120:123]\n"
    "ds_read_b128 a[224:227], v25 offset:58368\n"
    "buffer_load_dwordx4 v51, s[44:47], s96 offen offset:0 lds\n"
    "v_mfma_f32_16x16x32_bf16 v[120:123], a[40:43], a[212:215], v[120:123]\n"
    "ds_read_b128 a[228:231], v26 offset:58368\n"
    "v_mfma_f32_16x16x32_bf16 v[120:123], a[48:51], a[216:219], v[120:123]\n"
    "ds_read_b128 a[232:235], v27 offset:58368\n"
    "v_mfma_f32_16x16x32_bf16 v[120:123], a[56:59], a[220:223], v[120:123]\n"
    "s_waitcnt lgkmcnt(6)\n"
    "ds_read_b128 a[236:239], v28 offset:58368\n"
    "buffer_load_dwordx4 v52, s[44:47], s96 offen offset:1024 lds\n"
    "v_mfma_f32_16x16x32_bf16 v[124:127], a[60:63], a[220:223], v[124:127]\n"
    "s_add_u32 m0, s57, 32768\n"
    "ds_read_b128 a[240:243], v29 offset:0\n"
    "v_add_u32_dpp v19, v10, v2 row_newbcast:3 row_mask:0xf bank_mask:0xf\n"
    "v_mfma_f32_16x16x32_bf16 v[124:127], a[52:55], a[216:219], v[124:127]\n"
    "s_waitcnt lgkmcnt(6)\n"
    "ds_read_b128 a[244:247], v30 offset:0\n"
    "v_mfma_f32_16x16x32_bf16 v[124:127], a[44:47], a[212:215], v[124:127]\n"
    "s_waitcnt lgkmcnt(6)\n"
    "ds_read_b128 a[248:251], v31 offset:0\n"
    "v_mfma_f32_16x16x32_bf16 v[124:127], a[36:39], a[208:211], v[124:127]\n"
    "s_waitcnt lgkmcnt(6)\n"
    "ds_read_b128 a[252:255], v32 offset:0\n"
    "buffer_load_dwordx4 v19, s[48:51], 0 offen offset:0 lds\n"
    "v_mfma_f32_16x16x32_bf16 v[132:135], a[36:39], a[224:227], v[132:135]\n"
    "s_waitcnt lgkmcnt(6)\n"
    "ds_read_b128 v[232:235], v29 offset:1024\n"
    "v_add_u32_dpp v20, v10, v3 row_newbcast:11 row_mask:0xf bank_mask:0xf\n"
    "v_mfma_f32_16x16x32_bf16 v[132:135], a[44:47], a[228:231], v[132:135]\n"
    "s_waitcnt lgkmcnt(6)\n"
    "ds_read_b128 v[236:239], v30 offset:1024\n"
    "v_mfma_f32_16x16x32_bf16 v[132:135], a[52:55], a[232:235], v[132:135]\n"
    "s_waitcnt lgkmcnt(6)\n"
    "ds_read_b128 v[240:243], v31 offset:1024\n"
    "v_mfma_f32_16x16x32_bf16 v[132:135], a[60:63], a[236:239], v[132:135]\n"
    "ds_read_b128 v[244:247], v32 offset:1024\n"
    "buffer_load_dwordx4 v20, s[48:51], 0 offen offset:1024 lds\n"
    "v_mfma_f32_16x16x32_bf16 v[128:131], a[56:59], a[236:239], v[128:131]\n"
    "s_add_u32 m0, s57, 40960\n"
    "s_xor_b32 s96, s96, 98304\n"
    "v_mfma_f32_16x16x32_bf16 v[128:131], a[48:51], a[232:235], v[128:131]\n"
    "v_mfma_f32_16x16x32_bf16 v[128:131], a[40:43], a[228:231], v[128:131]\n"
    "v_mfma_f32_16x16x32_bf16 v[128:131], a[32:35], a[224:227], v[128:131]\n"
    "buffer_load_dwordx4 v51, s[44:47], s96 offen offset:0 lds\n"
    "buffer_load_dwordx4 v52, s[44:47], s96 offen offset:1024 lds\n"
    "s_add_u32 m0, s57, 49152\n"
    "s_xor_b32 s96, s96, 32768\n"
    "buffer_load_dwordx4 v51, s[44:47], s96 offen offset:0 lds\n"
    "buffer_load_dwordx4 v52, s[44:47], s96 offen offset:1024 lds\n"
    "s_add_u32 m0, s57, 57344\n"
    "v_add_u32_dpp v17, v10, v2 row_newbcast:2 row_mask:0xf bank_mask:0xf\n"
    "buffer_load_dwordx4 v17, s[48:51], 0 offen offset:0 lds\n"
    "v_add_u32_dpp v18, v10, v3 row_newbcast:10 row_mask:0xf bank_mask:0xf\n"
    "buffer_load_dwordx4 v18, s[48:51], 0 offen offset:1024 lds\n"
    "s_add_u32 m0, s57, 73728\n"
    "v_add_u32_dpp v19, v9, v2 row_newbcast:3 row_mask:0xf bank_mask:0xf\n"
    "buffer_load_dwordx4 v19, s[48:51], 0 offen offset:0 lds\n"
    "v_add_u32_dpp v20, v9, v3 row_newbcast:11 row_mask:0xf bank_mask:0xf\n"
    "buffer_load_dwordx4 v20, s[48:51], 0 offen offset:1024 lds\n"
    "s_waitcnt vmcnt(8) lgkmcnt(0)\n"
    "s_barrier\n"
    "ds_read_b128 a[208:211], v29 offset:8192\n"
    "ds_read_b128 a[212:215], v30 offset:8192\n"
    "v_mfma_f32_16x16x32_bf16 v[104:107], a[32:35], a[240:243], v[104:107]\n"
    "s_add_u32 m0, s57, 81920\n"
    "ds_read_b128 a[216:219], v31 offset:8192\n"
    "ds_read_b128 a[220:223], v32 offset:8192\n"
    "v_mfma_f32_16x16x32_bf16 v[104:107], a[40:43], a[244:247], v[104:107]\n"
    "ds_read_b128 a[224:227], v29 offset:9216\n"
    "ds_read_b128 a[228:231], v30 offset:9216\n"
    "v_mfma_f32_16x16x32_bf16 v[104:107], a[48:51], a[248:251], v[104:107]\n"
    "ds_read_b128 a[232:235], v31 offset:9216\n"
    "v_add_u32_dpp v17, v11, v2 row_newbcast:1 row_mask:0xf bank_mask:0xf\n"
    "v_mfma_f32_16x16x32_bf16 v[104:107], a[56:59], a[252:255], v[104:107]\n"
    "ds_read_b128 a[236:239], v32 offset:9216\n"
    "buffer_load_dwordx4 v17, s[48:51], 0 offen offset:0 lds\n"
    "v_mfma_f32_16x16x32_bf16 v[108:111], a[60:63], a[252:255], v[108:111]\n"
    "ds_read_b64_tr_b16 a[96:97], v49 offset:16384\n"
    "v_add_u32_dpp v18, v11, v3 row_newbcast:9 row_mask:0xf bank_mask:0xf\n"
    "v_mfma_f32_16x16x32_bf16 v[108:111], a[52:55], a[248:251], v[108:111]\n"
    "ds_read_b64_tr_b16 a[98:99], v49 offset:16896\n"
    "buffer_load_dwordx4 v18, s[48:51], 0 offen offset:1024 lds\n"
    "v_mfma_f32_16x16x32_bf16 v[108:111], a[44:47], a[244:247], v[108:111]\n"
    "s_add_u32 m0, s57, 65536\n"
    "ds_read_b64_tr_b16 a[104:105], v49 offset:24576\n"
    "v_add_u32_dpp v19, v13, v2 row_newbcast:1 row_mask:0xf bank_mask:0xf\n"
    "v_mfma_f32_16x16x32_bf16 v[108:111], a[36:39], a[240:243], v[108:111]\n"
    "ds_read_b128 a[240:243], v21 offset:32768\n"
    "v_mfma_f32_16x16x32_bf16 v[116:119], a[36:39], v[232:235], v[116:119]\n"
    "ds_read_b64_tr_b16 a[106:107], v49 offset:25088\n"
    "buffer_load_dwordx4 v19, s[48:51], 0 offen offset:0 lds\n"
    "v_mfma_f32_16x16x32_bf16 v[116:119], a[44:47], v[236:239], v[116:119]\n"
    "ds_read_b128 a[244:247], v22 offset:32768\n"
    "v_add_u32_dpp v20, v13, v3 row_newbcast:9 row_mask:0xf bank_mask:0xf\n"
    "v_mfma_f32_16x16x32_bf16 v[116:119], a[52:55], v[240:243], v[116:119]\n"
    "ds_read_b64_tr_b16 a[112:113], v45 offset:0\n"
    "v_mfma_f32_16x16x32_bf16 v[116:119], a[60:63], v[244:247], v[116:119]\n"
    "ds_read_b64_tr_b16 a[114:115], v45 offset:512\n"
    "v_mfma_f32_16x16x32_bf16 v[112:115], a[56:59], v[244:247], v[112:115]\n"
    "ds_read_b128 a[248:251], v23 offset:32768\n"
    "buffer_load_dwordx4 v20, s[48:51], 0 offen offset:1024 lds\n"
    "v_mfma_f32_16x16x32_bf16 v[112:115], a[48:51], v[240:243], v[112:115]\n"
    "s_add_u32 m0, s57, 90112\n"
    "ds_read_b64_tr_b16 a[120:121], v45 offset:16384\n"
    "v_add_u32_dpp v17, v9, v2 row_newbcast:2 row_mask:0xf bank_mask:0xf\n"
    "v_mfma_f32_16x16x32_bf16 v[112:115], a[40:43], v[236:239], v[112:115]\n"
    "ds_read_b64_tr_b16 a[122:123], v45 offset:16896\n"
    "v_mfma_f32_16x16x32_bf16 v[112:115], a[32:35], v[232:235], v[112:115]\n"
    "s_waitcnt lgkmcnt(6)\n"
    "ds_read_b128 a[252:255], v24 offset:32768\n"
    "v_mfma_f32_16x16x32_bf16 v[152:155], a[32:35], a[208:211], v[152:155]\n"
    "ds_read_b64_tr_b16 a[124:125], v46 offset:16384\n"
    "buffer_load_dwordx4 v17, s[48:51], 0 offen offset:0 lds\n"
    "v_mfma_f32_16x16x32_bf16 v[152:155], a[40:43], a[212:215], v[152:155]\n"
    "ds_read_b64_tr_b16 a[126:127], v46 offset:16896\n"
    "v_add_u32_dpp v18, v9, v3 row_newbcast:10 row_mask:0xf bank_mask:0xf\n"
    "v_mfma_f32_16x16x32_bf16 v[152:155], a[48:51], a[216:219], v[152:155]\n"
    "ds_read_b64_tr_b16 a[116:117], v46 offset:0\n"
    "v_mfma_f32_16x16x32_bf16 v[152:155], a[56:59], a[220:223], v[152:155]\n"
    "ds_read_b64_tr_b16 a[118:119], v46 offset:512\n"
    "buffer_load_dwordx4 v18, s[48:51], 0 offen offset:1024 lds\n"
    "v_mfma_f32_16x16x32_bf16 v[156:159], a[60:63], a[220:223], v[156:159]\n"
    "s_add_u32 m0, s57, 98304\n"
    "ds_read_b64_tr_b16 a[108:109], v50 offset:24576\n"
    "v_readlane_b32 s96, v8, 5\n"
    "v_mfma_f32_16x16x32_bf16 v[156:159], a[52:55], a[216:219], v[156:159]\n"
    "s_xor_b32 s96, s96, 32768\n"
    "ds_read_b64_tr_b16 a[110:111], v50 offset:25088\n"
    "v_mfma_f32_16x16x32_bf16 v[156:159], a[44:47], a[212:215], v[156:159]\n"
    "ds_read_b64_tr_b16 a[100:101], v50 offset:16384\n"
    "v_mfma_f32_16x16x32_bf16 v[156:159], a[36:39], a[208:211], v[156:159]\n"
    "ds_read_b64_tr_b16 a[102:103], v50 offset:16896\n"
    "buffer_load_dwordx4 v51, s[44:47], s96 offen offset:0 lds\n"
    "v_mfma_f32_16x16x32_bf16 v[164:167], a[36:39], a[224:227], v[164:167]\n"
    "ds_read_b128 v[232:235], v21 offset:33792\n"
    "v_mfma_f32_16x16x32_bf16 v[164:167], a[44:47], a[228:231], v[164:167]\n"
    "ds_read_b128 v[236:239], v22 offset:33792\n"
    "v_mfma_f32_16x16x32_bf16 v[164:167], a[52:55], a[232:235], v[164:167]\n"
    "ds_read_b128 v[240:243], v23 offset:33792\n"
    "v_mfma_f32_16x16x32_bf16 v[164:167], a[60:63], a[236:239], v[164:167]\n"
    "ds_read_b128 v[244:247], v24 offset:33792\n"
    "buffer_load_dwordx4 v52, s[44:47], s96 offen offset:1024 lds\n"
    "v_mfma_f32_16x16x32_bf16 v[160:163], a[56:59], a[236:239], v[160:163]\n"
    "s_add_u32 m0, s57, 106496\n"
    "s_xor_b32 s96, s96, 98304\n"
    "v_mfma_f32_16x16x32_bf16 v[160:163], a[48:51], a[232:235], v[160:163]\n"
    "v_mfma_f32_16x16x32_bf16 v[160:163], a[40:43], a[228:231], v[160:163]\n"
    "v_mfma_f32_16x16x32_bf16 v[160:163], a[32:35], a[224:227], v[160:163]\n"
    "buffer_load_dwordx4 v51, s[44:47], s96 offen offset:0 lds\n"
    "s_waitcnt vmcnt(9) lgkmcnt(0)\n"
    "s_barrier\n"
    "ds_read_b64_tr_b16 a[64:65], v45 offset:8192\n"
    "ds_read_b64_tr_b16 a[66:67], v45 offset:8704\n"
    "v_mfma_f32_16x16x32_bf16 v[120:123], a[96:99], a[240:243], v[120:123]\n"
    "ds_read_b128 a[208:211], v21 offset:57344\n"
    "ds_read_b64_tr_b16 a[72:73], v45 offset:24576\n"
    "v_mfma_f32_16x16x32_bf16 v[120:123], a[104:107], a[244:247], v[120:123]\n"
    "ds_read_b64_tr_b16 a[74:75], v45 offset:25088\n"
    "ds_read_b128 a[212:215], v22 offset:57344\n"
    "v_mfma_f32_16x16x32_bf16 v[120:123], a[112:115], a[248:251], v[120:123]\n"
    "ds_read_b64_tr_b16 a[80:81], v45 offset:40960\n"
    "buffer_load_dwordx4 v52, s[44:47], s96 offen offset:1024 lds\n"
    "v_mfma_f32_16x16x32_bf16 v[120:123], a[120:123], a[252:255], v[120:123]\n"
    "s_add_u32 m0, s57, 114688\n"
    "ds_read_b64_tr_b16 a[82:83], v45 offset:41472\n"
    "s_xor_b32 s96, s96, 32768\n"
    "v_mfma_f32_16x16x32_bf16 v[124:127], a[124:127], a[252:255], v[124:127]\n"
    "ds_read_b128 a[216:219], v23 offset:57344\n"
    "buffer_load_dwordx4 v51, s[44:47], s96 offen offset:0 lds\n"
    "v_mfma_f32_16x16x32_bf16 v[124:127], a[116:119], a[248:251], v[124:127]\n"
    "ds_read_b64_tr_b16 a[88:89], v45 offset:49152\n"
    "v_mfma_f32_16x16x32_bf16 v[124:127], a[108:111], a[244:247], v[124:127]\n"
    "ds_read_b64_tr_b16 a[90:91], v45 offset:49664\n"
    "v_mfma_f32_16x16x32_bf16 v[124:127], a[100:103], a[240:243], v[124:127]\n"
    "ds_read_b128 a[220:223], v24 offset:57344\n"
    "buffer_load_dwordx4 v52, s[44:47], s96 offen offset:1024 lds\n"
    "v_mfma_f32_16x16x32_bf16 v[132:135], a[100:103], v[232:235], v[132:135]\n"
    "s_add_u32 m0, s57, 122880\n"
    "ds_read_b64_tr_b16 a[92:93], v46 offset:49152\n"
    "v_add_u32_dpp v19, v9, v2 row_newbcast:5 row_mask:0xf bank_mask:0xf\n"
    "v_mfma_f32_16x16x32_bf16 v[132:135], a[108:111], v[236:239], v[132:135]\n"
    "ds_read_b64_tr_b16 a[94:95], v46 offset:49664\n"
    "v_mfma_f32_16x16x32_bf16 v[132:135], a[116:119], v[240:243], v[132:135]\n"
    "ds_read_b64_tr_b16 a[84:85], v46 offset:40960\n"
    "v_mfma_f32_16x16x32_bf16 v[132:135], a[124:127], v[244:247], v[132:135]\n"
    "ds_read_b64_tr_b16 a[86:87], v46 offset:41472\n"
    "buffer_load_dwordx4 v19, s[48:51], 0 offen offset:0 lds\n"
    "v_mfma_f32_16x16x32_bf16 v[128:131], a[120:123], v[244:247], v[128:131]\n"
    "ds_read_b64_tr_b16 a[76:77], v46 offset:24576\n"
    "v_add_u32_dpp v20, v9, v3 row_newbcast:13 row_mask:0xf bank_mask:0xf\n"
    "v_mfma_f32_16x16x32_bf16 v[128:131], a[112:115], v[240:243], v[128:131]\n"
    "ds_read_b64_tr_b16 a[78:79], v46 offset:25088\n"
    "v_mfma_f32_16x16x32_bf16 v[128:131], a[104:107], v[236:239], v[128:131]\n"
    "ds_read_b64_tr_b16 a[68:69], v46 offset:8192\n"
    "v_mfma_f32_16x16x32_bf16 v[128:131], a[96:99], v[232:235], v[128:131]\n"
    "s_waitcnt lgkmcnt(6)\n"
    "ds_read_b64_tr_b16 a[70:71], v46 offset:8704\n"
    "buffer_load_dwordx4 v20, s[48:51], 0 offen offset:1024 lds\n"
    "v_mfma_f32_16x16x32_bf16 v[120:123], a[64:67], a[208:211], v[120:123]\n"
    "ds_read_b128 a[224:227], v21 offset:58368\n"
    "v_mfma_f32_16x16x32_bf16 v[120:123], a[72:75], a[212:215], v[120:123]\n"
    "ds_read_b128 a[228:231], v22 offset:58368\n"
    "v_mfma_f32_16x16x32_bf16 v[120:123], a[80:83], a[216:219], v[120:123]\n"
    "ds_read_b128 a[232:235], v23 offset:58368\n"
    "v_mfma_f32_16x16x32_bf16 v[120:123], a[88:91], a[220:223], v[120:123]\n"
    "s_waitcnt lgkmcnt(6)\n"
    "ds_read_b128 a[236:239], v24 offset:58368\n"
    "buffer_load_dword v16, v7, s[40:43], s37 offen\n"
    "v_mfma_f32_16x16x32_bf16 v[124:127], a[92:95], a[220:223], v[124:127]\n"
    "ds_read_b128 a[240:243], v25 offset:8192\n"
    "v_mfma_f32_16x16x32_bf16 v[124:127], a[84:87], a[216:219], v[124:127]\n"
    "s_waitcnt lgkmcnt(6)\n"
    "ds_read_b128 a[244:247], v26 offset:8192\n"
    "v_mfma_f32_16x16x32_bf16 v[124:127], a[76:79], a[212:215], v[124:127]\n"
    "s_waitcnt lgkmcnt(6)\n"
    "ds_read_b128 a[248:251], v27 offset:8192\n"
    "v_mfma_f32_16x16x32_bf16 v[124:127], a[68:71], a[208:211], v[124:127]\n"
    "s_waitcnt lgkmcnt(6)\n"
    "ds_read_b128 a[252:255], v28 offset:8192\n"
    "buffer_load_dword v15, v6, s[40:43], s37 offen\n"
    "v_mfma_f32_16x16x32_bf16 v[132:135], a[68:71], a[224:227], v[132:135]\n"
    "s_waitcnt lgkmcnt(6)\n"
    "ds_read_b128 v[232:235], v25 offset:9216\n"
    "s_add_u32 m0, s57, 131072\n"
    "v_mfma_f32_16x16x32_bf16 v[132:135], a[76:79], a[228:231], v[132:135]\n"
    "s_waitcnt lgkmcnt(6)\n"
    "ds_read_b128 v[236:239], v26 offset:9216\n"
    "v_add_u32_dpp v17, v10, v2 row_newbcast:5 row_mask:0xf bank_mask:0xf\n"
    "v_mfma_f32_16x16x32_bf16 v[132:135], a[84:87], a[232:235], v[132:135]\n"
    "s_waitcnt lgkmcnt(6)\n"
    "ds_read_b128 v[240:243], v27 offset:9216\n"
    "v_mfma_f32_16x16x32_bf16 v[132:135], a[92:95], a[236:239], v[132:135]\n"
    "ds_read_b128 v[244:247], v28 offset:9216\n"
    "buffer_load_dwordx4 v17, s[48:51], 0 offen offset:0 lds\n"
    "v_mfma_f32_16x16x32_bf16 v[128:131], a[88:91], a[236:239], v[128:131]\n"
    "v_add_u32_dpp v18, v10, v3 row_newbcast:13 row_mask:0xf bank_mask:0xf\n"
    "v_mfma_f32_16x16x32_bf16 v[128:131], a[80:83], a[232:235], v[128:131]\n"
    "v_mfma_f32_16x16x32_bf16 v[128:131], a[72:75], a[228:231], v[128:131]\n"
    "v_mfma_f32_16x16x32_bf16 v[128:131], a[64:67], a[224:227], v[128:131]\n"
    "buffer_load_dwordx4 v18, s[48:51], 0 offen offset:1024 lds\n"
    "s_waitcnt vmcnt(14) lgkmcnt(0)\n"
    "s_barrier\n"
    "v_mfma_f32_16x16x32_bf16 v[104:107], a[96:99], a[240:243], v[104:107]\n"
    "s_add_u32 m0, s57, 139264\n"
    "ds_read_b128 a[208:211], v25 offset:16384\n"
    "v_add_u32_dpp v19, v14, v2 row_newbcast:1 row_mask:0xf bank_mask:0xf\n"
    "v_mfma_f32_16x16x32_bf16 v[104:107], a[104:107], a[244:247], v[104:107]\n"
    "s_mov_b32 s100, 0\n"
    "ds_read_b128 a[212:215], v26 offset:16384\n"
    "buffer_load_dwordx4 v19, s[48:51], 0 offen offset:0 lds\n"
    "v_mfma_f32_16x16x32_bf16 v[104:107], a[112:115], a[248:251], v[104:107]\n"
    "s_mov_b32 s101, 0x4d000000\n"
    "ds_read_b128 a[216:219], v27 offset:16384\n"
    "v_add_u32_dpp v20, v14, v3 row_newbcast:9 row_mask:0xf bank_mask:0xf\n"
    "v_mfma_f32_16x16x32_bf16 v[104:107], a[120:123], a[252:255], v[104:107]\n"
    "s_xor_b32 s34, %1, 1\n"
    "ds_read_b128 a[220:223], v28 offset:16384\n"
    "s_mul_i32 s34, s34, 49152\n"
    "v_mfma_f32_16x16x32_bf16 v[108:111], a[124:127], a[252:255], v[108:111]\n"
    "s_xor_b32 s35, %1, 2\n"
    "ds_read_b128 a[224:227], v25 offset:17408\n"
    "s_mul_i32 s35, s35, 49152\n"
    "v_mfma_f32_16x16x32_bf16 v[108:111], a[116:119], a[248:251], v[108:111]\n"
    "s_add_u32 s35, s35, 16384\n"
    "ds_read_b128 a[228:231], v26 offset:17408\n"
    "buffer_load_dwordx4 v20, s[48:51], 0 offen offset:1024 lds\n"
    "v_mfma_f32_16x16x32_bf16 v[108:111], a[108:111], a[244:247], v[108:111]\n"
    "s_add_u32 m0, s57, 147456\n"
    "ds_read_b128 a[232:235], v27 offset:17408\n"
    "v_readlane_b32 s96, v8, 4\n"
    "v_mfma_f32_16x16x32_bf16 v[108:111], a[100:103], a[240:243], v[108:111]\n"
    "s_xor_b32 s96, s96, 0\n"
    "ds_read_b128 a[236:239], v28 offset:17408\n"
    "s_xor_b32 s36, %1, 3\n"
    "v_mfma_f32_16x16x32_bf16 v[116:119], a[100:103], v[232:235], v[116:119]\n"
    "s_mul_i32 s36, s36, 49152\n"
    "ds_read_b128 a[240:243], v25 offset:0\n"
    "s_add_u32 s36, s36, 32768\n"
    "v_mfma_f32_16x16x32_bf16 v[116:119], a[108:111], v[236:239], v[116:119]\n"
    "ds_read_b128 a[244:247], v26 offset:0\n"
    "buffer_load_dwordx4 v51, s[44:47], s96 offen offset:0 lds\n"
    "v_mfma_f32_16x16x32_bf16 v[116:119], a[116:119], v[240:243], v[116:119]\n"
    "ds_read_b128 a[248:251], v27 offset:0\n"
    "v_mfma_f32_16x16x32_bf16 v[116:119], a[124:127], v[244:247], v[116:119]\n"
    "ds_read_b128 a[252:255], v28 offset:0\n"
    "v_mfma_f32_16x16x32_bf16 v[112:115], a[120:123], v[244:247], v[112:115]\n"
    "v_mfma_f32_16x16x32_bf16 v[112:115], a[112:115], v[240:243], v[112:115]\n"
    "buffer_load_dwordx4 v52, s[44:47], s96 offen offset:1024 lds\n"
    "s_add_u32 m0, s57, 155648\n"
    "v_mfma_f32_16x16x32_bf16 v[112:115], a[104:107], v[236:239], v[112:115]\n"
    "s_xor_b32 s96, s96, 32768\n"
    "v_mfma_f32_16x16x32_bf16 v[112:115], a[96:99], v[232:235], v[112:115]\n"
    "s_waitcnt lgkmcnt(3)\n"
    "ds_read_b128 v[232:235], v25 offset:1024\n"
    "buffer_load_dwordx4 v51, s[44:47], s96 offen offset:0 lds\n"
    "v_mfma_f32_16x16x32_bf16 v[136:139], a[32:35], a[208:211], v[136:139]\n"
    "ds_read_b128 v[236:239], v26 offset:1024\n"
    "v_mfma_f32_16x16x32_bf16 v[136:139], a[40:43], a[212:215], v[136:139]\n"
    "ds_read_b128 v[240:243], v27 offset:1024\n"
    "v_mfma_f32_16x16x32_bf16 v[136:139], a[48:51], a[216:219], v[136:139]\n"
    "ds_read_b128 v[244:247], v28 offset:1024\n"
    "buffer_load_dwordx4 v52, s[44:47], s96 offen offset:1024 lds\n"
    "v_mfma_f32_16x16x32_bf16 v[136:139], a[56:59], a[220:223], v[136:139]\n"
    "s_add_u32 m0, s57, 0\n"
    "s_xor_b32 s96, s96, 98304\n"
    "v_mfma_f32_16x16x32_bf16 v[140:143], a[60:63], a[220:223], v[140:143]\n"
    "v_mfma_f32_16x16x32_bf16 v[140:143], a[52:55], a[216:219], v[140:143]\n"
    "v_mfma_f32_16x16x32_bf16 v[140:143], a[44:47], a[212:215], v[140:143]\n"
    "buffer_load_dwordx4 v51, s[44:47], s96 offen offset:0 lds\n"
    "v_mfma_f32_16x16x32_bf16 v[140:143], a[36:39], a[208:211], v[140:143]\n"
    "v_mfma_f32_16x16x32_bf16 v[148:151], a[36:39], a[224:227], v[148:151]\n"
    "v_mfma_f32_16x16x32_bf16 v[148:151], a[44:47], a[228:231], v[148:151]\n"
    "v_mfma_f32_16x16x32_bf16 v[148:151], a[52:55], a[232:235], v[148:151]\n"
    "buffer_load_dwordx4 v52, s[44:47], s96 offen offset:1024 lds\n"
    "v_mfma_f32_16x16x32_bf16 v[148:151], a[60:63], a[236:239], v[148:151]\n"
    "s_add_u32 m0, s57, 16384\n"
    "s_xor_b32 s96, s96, 32768\n"
    "v_mfma_f32_16x16x32_bf16 v[144:147], a[56:59], a[236:239], v[144:147]\n"
    "v_mfma_f32_16x16x32_bf16 v[144:147], a[48:51], a[232:235], v[144:147]\n"
    "v_mfma_f32_16x16x32_bf16 v[144:147], a[40:43], a[228:231], v[144:147]\n"
    "buffer_load_dwordx4 v51, s[44:47], s96 offen offset:0 lds\n"
    "v_mfma_f32_16x16x32_bf16 v[144:147], a[32:35], a[224:227], v[144:147]\n"
    "s_waitcnt vmcnt(13) lgkmcnt(0)\n"
    "s_barrier\n"
    "v_mfma_f32_16x16x32_bf16 v[168:171], a[32:35], a[240:243], v[168:171]\n"
    "ds_read_b128 a[208:211], v25 offset:24576\n"
    "buffer_load_dwordx4 v52, s[44:47], s96 offen offset:1024 lds\n"
    "v_mfma_f32_16x16x32_bf16 v[168:171], a[40:43], a[244:247], v[168:171]\n"
    "s_add_u32 m0, s57, 32768\n"
    "ds_read_b128 a[212:215], v26 offset:24576\n"
    "v_add_u32_dpp v17, v9, v2 row_newbcast:4 row_mask:0xf bank_mask:0xf\n"
    "v_mfma_f32_16x16x32_bf16 v[168:171], a[48:51], a[248:251], v[168:171]\n"
    "ds_read_b128 a[216:219], v27 offset:24576\n"
    "v_mfma_f32_16x16x32_bf16 v[168:171], a[56:59], a[252:255], v[168:171]\n"
    "ds_read_b128 a[220:223], v28 offset:24576\n"
    "v_mfma_f32_16x16x32_bf16 v[172:175], a[60:63], a[252:255], v[172:175]\n"
    "ds_read_b128 a[224:227], v25 offset:25600\n"
    "buffer_load_dwordx4 v17, s[48:51], 0 offen offset:0 lds\n"
    "v_mfma_f32_16x16x32_bf16 v[172:175], a[52:55], a[248:251], v[172:175]\n"
    "ds_read_b128 a[228:231], v26 offset:25600\n"
    "v_add_u32_dpp v18, v9, v3 row_newbcast:12 row_mask:0xf bank_mask:0xf\n"
    "v_mfma_f32_16x16x32_bf16 v[172:175], a[44:47], a[244:247], v[172:175]\n"
    "ds_read_b128 a[232:235], v27 offset:25600\n"
    "v_mfma_f32_16x16x32_bf16 v[172:175], a[36:39], a[240:243], v[172:175]\n"
    "ds_read_b128 a[236:239], v28 offset:25600\n"
    "v_mfma_f32_16x16x32_bf16 v[180:183], a[36:39], v[232:235], v[180:183]\n"
    "ds_read_b128 a[240:243], v25 offset:57344\n"
    "buffer_load_dwordx4 v18, s[48:51], 0 offen offset:1024 lds\n"
    "v_mfma_f32_16x16x32_bf16 v[180:183], a[44:47], v[236:239], v[180:183]\n"
    "s_add_u32 m0, s57, 8192\n"
    "ds_read_b64_tr_b16 a[168:169], v47 offset:32768\n"
    "v_add_u32_dpp v19, v10, v2 row_newbcast:4 row_mask:0xf bank_mask:0xf\n"
    "v_mfma_f32_16x16x32_bf16 v[180:183], a[52:55], v[240:243], v[180:183]\n"
    "ds_read_b64_tr_b16 a[170:171], v47 offset:33280\n"
    "v_mfma_f32_16x16x32_bf16 v[180:183], a[60:63], v[244:247], v[180:183]\n"
    "ds_read_b128 a[244:247], v26 offset:57344\n"
    "v_mfma_f32_16x16x32_bf16 v[176:179], a[56:59], v[244:247], v[176:179]\n"
    "ds_read_b64_tr_b16 a[176:177], v47 offset:40960\n"
    "buffer_load_dwordx4 v19, s[48:51], 0 offen offset:0 lds\n"
    "v_mfma_f32_16x16x32_bf16 v[176:179], a[48:51], v[240:243], v[176:179]\n"
    "ds_read_b64_tr_b16 a[178:179], v47 offset:41472\n"
    "v_add_u32_dpp v20, v10, v3 row_newbcast:12 row_mask:0xf bank_mask:0xf\n"
    "v_mfma_f32_16x16x32_bf16 v[176:179], a[40:43], v[236:239], v[176:179]\n"
    "ds_read_b128 a[248:251], v27 offset:57344\n"
    "v_mfma_f32_16x16x32_bf16 v[176:179], a[32:35], v[232:235], v[176:179]\n"
    "s_waitcnt lgkmcnt(6)\n"
    "ds_read_b64_tr_b16 a[184:185], v47 offset:49152\n"
    "v_mfma_f32_16x16x32_bf16 v[104:107], a[64:67], a[208:211], v[104:107]\n"
    "ds_read_b64_tr_b16 a[186:187], v47 offset:49664\n"
    "buffer_load_dwordx4 v20, s[48:51], 0 offen offset:1024 lds\n"
    "v_mfma_f32_16x16x32_bf16 v[104:107], a[72:75], a[212:215], v[104:107]\n"
    "s_add_u32 m0, s57, 24576\n"
    "ds_read_b128 a[252:255], v28 offset:57344\n"
    "v_add_u32_dpp v17, v11, v2 row_newbcast:2 row_mask:0xf bank_mask:0xf\n"
    "v_mfma_f32_16x16x32_bf16 v[104:107], a[80:83], a[216:219], v[104:107]\n"
    "ds_read_b64_tr_b16 a[188:189], v48 offset:49152\n"
    "v_mfma_f32_16x16x32_bf16 v[104:107], a[88:91], a[220:223], v[104:107]\n"
    "ds_read_b64_tr_b16 a[190:191], v48 offset:49664\n"
    "buffer_load_dwordx4 v17, s[48:51], 0 offen offset:0 lds\n"
    "v_mfma_f32_16x16x32_bf16 v[108:111], a[92:95], a[220:223], v[108:111]\n"
    "ds_read_b64_tr_b16 a[180:181], v48 offset:40960\n"
    "v_add_u32_dpp v18, v11, v3 row_newbcast:10 row_mask:0xf bank_mask:0xf\n"
    "v_mfma_f32_16x16x32_bf16 v[108:111], a[84:87], a[216:219], v[108:111]\n"
    "ds_read_b64_tr_b16 a[182:183], v48 offset:41472\n"
    "v_mfma_f32_16x16x32_bf16 v[108:111], a[76:79], a[212:215], v[108:111]\n"
    "ds_read_b64_tr_b16 a[172:173], v48 offset:32768\n"
    "v_mfma_f32_16x16x32_bf16 v[108:111], a[68:71], a[208:211], v[108:111]\n"
    "ds_read_b64_tr_b16 a[174:175], v48 offset:33280\n"
    "buffer_load_dwordx4 v18, s[48:51], 0 offen offset:1024 lds\n"
    "v_mfma_f32_16x16x32_bf16 v[116:119], a[68:71], a[224:227], v[116:119]\n"
    "s_add_u32 m0, s57, 40960\n"
    "ds_read_b128 v[232:235], v25 offset:58368\n"
    "v_add_u32_dpp v19, v12, v2 row_newbcast:3 row_mask:0xf bank_mask:0xf\n"
    "v_mfma_f32_16x16x32_bf16 v[116:119], a[76:79], a[228:231], v[116:119]\n"
    "ds_read_b128 v[236:239], v26 offset:58368\n"
    "v_mfma_f32_16x16x32_bf16 v[116:119], a[84:87], a[232:235], v[116:119]\n"
    "ds_read_b128 v[240:243], v27 offset:58368\n"
    "v_mfma_f32_16x16x32_bf16 v[116:119], a[92:95], a[236:239], v[116:119]\n"
    "ds_read_b128 v[244:247], v28 offset:58368\n"
    "buffer_load_dwordx4 v19, s[48:51], 0 offen offset:0 lds\n"
    "v_mfma_f32_16x16x32_bf16 v[112:115], a[88:91], a[236:239], v[112:115]\n"
    "v_add_u32_dpp v20, v12, v3 row_newbcast:11 row_mask:0xf bank_mask:0xf\n"
    "v_mfma_f32_16x16x32_bf16 v[112:115], a[80:83], a[232:235], v[112:115]\n"
    "v_mfma_f32_16x16x32_bf16 v[112:115], a[72:75], a[228:231], v[112:115]\n"
    "v_mfma_f32_16x16x32_bf16 v[112:115], a[64:67], a[224:227], v[112:115]\n"
    "buffer_load_dwordx4 v20, s[48:51], 0 offen offset:1024 lds\n"
    "s_waitcnt vmcnt(14) lgkmcnt(0)\n"
    "s_barrier\n"
    "v_mfma_f32_16x16x32_bf16 v[104:107], a[160:163], a[240:243], v[104:107]\n"
    "s_add_u32 m0, s57, 49152\n"
    "ds_read_b128 a[208:211], v29 offset:0\n"
    "v_readlane_b32 s96, v8, 6\n"
    "v_mfma_f32_16x16x32_bf16 v[104:107], a[168:171], a[244:247], v[104:107]\n"
    "s_xor_b32 s96, s96, 0\n"
    "ds_read_b128 a[212:215], v30 offset:0\n"
    "v_lshlrev_b32 v16, 8, v16\n"
    "v_mfma_f32_16x16x32_bf16 v[104:107], a[176:179], a[248:251], v[104:107]\n"
    "ds_read_b128 a[216:219], v31 offset:0\n"
    "buffer_load_dwordx4 v51, s[44:47], s96 offen offset:0 lds\n"
    "v_mfma_f32_16x16x32_bf16 v[104:107], a[184:187], a[252:255], v[104:107]\n"
    "ds_read_b128 a[220:223], v32 offset:0\n"
    "v_lshlrev_b32 v15, 8, v15\n"
    "v_mfma_f32_16x16x32_bf16 v[108:111], a[188:191], a[252:255], v[108:111]\n"
    "ds_read_b128 a[224:227], v29 offset:1024\n"
    "v_mfma_f32_16x16x32_bf16 v[108:111], a[180:183], a[248:251], v[108:111]\n"
    "ds_read_b128 a[228:231], v30 offset:1024\n"
    "buffer_load_dwordx4 v52, s[44:47], s96 offen offset:1024 lds\n"
    "v_mfma_f32_16x16x32_bf16 v[108:111], a[172:175], a[244:247], v[108:111]\n"
    "s_add_u32 m0, s57, 57344\n"
    "ds_read_b128 a[232:235], v31 offset:1024\n"
    "s_xor_b32 s96, s96, 32768\n"
    "v_mfma_f32_16x16x32_bf16 v[108:111], a[164:167], a[240:243], v[108:111]\n"
    "ds_read_b128 a[236:239], v32 offset:1024\n"
    "v_mfma_f32_16x16x32_bf16 v[116:119], a[164:167], v[232:235], v[116:119]\n"
    "ds_read_b128 a[240:243], v29 offset:8192\n"
    "v_mfma_f32_16x16x32_bf16 v[116:119], a[172:175], v[236:239], v[116:119]\n"
    "ds_read_b128 a[244:247], v30 offset:8192\n"
    "buffer_load_dwordx4 v51, s[44:47], s96 offen offset:0 lds\n"
    "v_mfma_f32_16x16x32_bf16 v[116:119], a[180:183], v[240:243], v[116:119]\n"
    "ds_read_b128 a[248:251], v31 offset:8192\n"
    "v_mfma_f32_16x16x32_bf16 v[116:119], a[188:191], v[244:247], v[116:119]\n"
    "ds_read_b128 a[252:255], v32 offset:8192\n"
    "v_mfma_f32_16x16x32_bf16 v[112:115], a[184:187], v[244:247], v[112:115]\n"
    "ds_read_b64_tr_b16 a[128:129], v49 offset:16384\n"
    "v_mfma_f32_16x16x32_bf16 v[112:115], a[176:179], v[240:243], v[112:115]\n"
    "ds_read_b64_tr_b16 a[130:131], v49 offset:16896\n"
    "buffer_load_dwordx4 v52, s[44:47], s96 offen offset:1024 lds\n"
    "v_mfma_f32_16x16x32_bf16 v[112:115], a[168:171], v[236:239], v[112:115]\n"
    "s_add_u32 m0, s57, 73728\n"
    "ds_read_b64_tr_b16 a[132:133], v50 offset:16384\n"
    "v_add_u32_dpp v17, v9, v4 row_newbcast:6 row_mask:0x5 bank_mask:0x3\n"
    "v_mfma_f32_16x16x32_bf16 v[112:115], a[160:163], v[232:235], v[112:115]\n"
    "s_waitcnt lgkmcnt(6)\n"
    "ds_read_b128 v[232:235], v29 offset:9216\n"
    "v_add_u32_dpp v17, v9, v4 row_newbcast:6 row_mask:0xa bank_mask:0xc\n"
    "v_mfma_f32_16x16x32_bf16 v[120:123], a[160:163], a[208:211], v[120:123]\n"
    "ds_read_b128 v[236:239], v30 offset:9216\n"
    "v_add_u32_dpp v17, v11, v4 row_newbcast:6 row_mask:0x5 bank_mask:0xc\n"
    "v_mfma_f32_16x16x32_bf16 v[120:123], a[168:171], a[212:215], v[120:123]\n"
    "ds_read_b128 v[240:243], v31 offset:9216\n"
    "v_add_u32_dpp v17, v11, v4 row_newbcast:6 row_mask:0xa bank_mask:0x3\n"
    "v_mfma_f32_16x16x32_bf16 v[120:123], a[176:179], a[216:219], v[120:123]\n"
    "ds_read_b128 v[244:247], v32 offset:9216\n"
    "buffer_load_dwordx4 v17, s[48:51], 0 offen offset:0 lds\n"
    "v_mfma_f32_16x16x32_bf16 v[120:123], a[184:187], a[220:223], v[120:123]\n"
    "ds_read_b64_tr_b16 a[134:135], v50 offset:16896\n"
    "v_add_u32_dpp v18, v13, v5 row_newbcast:6 row_mask:0x5 bank_mask:0x3\n"
    "v_mfma_f32_16x16x32_bf16 v[124:127], a[188:191], a[220:223], v[124:127]\n"
    "v_add_u32_dpp v18, v13, v5 row_newbcast:6 row_mask:0xa bank_mask:0xc\n"
    "v_add_u32_dpp v18, v15, v5 row_newbcast:6 row_mask:0x5 bank_mask:0xc\n"
    "v_mfma_f32_16x16x32_bf16 v[124:127], a[180:183], a[216:219], v[124:127]\n"
    "v_add_u32_dpp v18, v15, v5 row_newbcast:6 row_mask:0xa bank_mask:0x3\n"
    "v_mfma_f32_16x16x32_bf16 v[124:127], a[172:175], a[212:215], v[124:127]\n"
    "buffer_load_dwordx4 v18, s[48:51], 0 offen offset:1024 lds\n"
    "s_add_u32 m0, s57, 81920\n"
    "v_mfma_f32_16x16x32_bf16 v[124:127], a[164:167], a[208:211], v[124:127]\n"
    "v_add_u32_dpp v19, v9, v4 row_newbcast:14 row_mask:0x5 bank_mask:0x3\n"
    "v_add_u32_dpp v19, v9, v4 row_newbcast:14 row_mask:0xa bank_mask:0xc\n"
    "v_mfma_f32_16x16x32_bf16 v[132:135], a[164:167], a[224:227], v[132:135]\n"
    "v_add_u32_dpp v19, v11, v4 row_newbcast:14 row_mask:0x5 bank_mask:0xc\n"
    "v_add_u32_dpp v19, v11, v4 row_newbcast:14 row_mask:0xa bank_mask:0x3\n"
    "v_mfma_f32_16x16x32_bf16 v[132:135], a[172:175], a[228:231], v[132:135]\n"
    "buffer_load_dwordx4 v19, s[48:51], 0 offen offset:0 lds\n"
    "v_add_u32_dpp v20, v13, v5 row_newbcast:14 row_mask:0x5 bank_mask:0x3\n"
    "v_mfma_f32_16x16x32_bf16 v[132:135], a[180:183], a[232:235], v[132:135]\n"
    "v_add_u32_dpp v20, v13, v5 row_newbcast:14 row_mask:0xa bank_mask:0xc\n"
    "v_add_u32_dpp v20, v15, v5 row_newbcast:14 row_mask:0x5 bank_mask:0xc\n"
    "v_mfma_f32_16x16x32_bf16 v[132:135], a[188:191], a[236:239], v[132:135]\n"
    "v_add_u32_dpp v20, v15, v5 row_newbcast:14 row_mask:0xa bank_mask:0x3\n"
    "v_mfma_f32_16x16x32_bf16 v[128:131], a[184:187], a[236:239], v[128:131]\n"
    "buffer_load_dwordx4 v20, s[48:51], 0 offen offset:1024 lds\n"
    "v_mfma_f32_16x16x32_bf16 v[128:131], a[176:179], a[232:235], v[128:131]\n"
    "s_add_u32 m0, s57, 65536\n"
    "v_add_u32_dpp v17, v10, v4 row_newbcast:6 row_mask:0x5 bank_mask:0x3\n"
    "v_add_u32_dpp v17, v10, v4 row_newbcast:6 row_mask:0xa bank_mask:0xc\n"
    "v_mfma_f32_16x16x32_bf16 v[128:131], a[168:171], a[228:231], v[128:131]\n"
    "v_add_u32_dpp v17, v12, v4 row_newbcast:6 row_mask:0x5 bank_mask:0xc\n"
    "v_add_u32_dpp v17, v12, v4 row_newbcast:6 row_mask:0xa bank_mask:0x3\n"
    "v_mfma_f32_16x16x32_bf16 v[128:131], a[160:163], a[224:227], v[128:131]\n"
    "s_waitcnt vmcnt(12) lgkmcnt(0)\n"
    "s_barrier\n"
    "v_mfma_f32_16x16x32_bf16 v[184:187], a[32:35], a[240:243], v[184:187]\n"
    "ds_read_b128 a[208:211], v21 offset:32768\n"
    "buffer_load_dwordx4 v17, s[48:51], 0 offen offset:0 lds\n"
    "v_mfma_f32_16x16x32_bf16 v[184:187], a[40:43], a[244:247], v[184:187]\n"
    "ds_read_b64_tr_b16 a[136:137], v49 offset:24576\n"
    "v_add_u32_dpp v18, v14, v5 row_newbcast:6 row_mask:0x5 bank_mask:0x3\n"
    "v_mfma_f32_16x16x32_bf16 v[184:187], a[48:51], a[248:251], v[184:187]\n"
    "ds_read_b64_tr_b16 a[138:139], v49 offset:25088\n"
    "v_add_u32_dpp v18, v14, v5 row_newbcast:6 row_mask:0xa bank_mask:0xc\n"
    "v_mfma_f32_16x16x32_bf16 v[184:187], a[56:59], a[252:255], v[184:187]\n"
    "ds_read_b128 a[212:215], v22 offset:32768\n"
    "v_add_u32_dpp v18, v16, v5 row_newbcast:6 row_mask:0x5 bank_mask:0xc\n"
    "v_mfma_f32_16x16x32_bf16 v[188:191], a[60:63], a[252:255], v[188:191]\n"
    "ds_read_b64_tr_b16 a[144:145], v45 offset:0\n"
    "v_add_u32_dpp v18, v16, v5 row_newbcast:6 row_mask:0xa bank_mask:0x3\n"
    "v_mfma_f32_16x16x32_bf16 v[188:191], a[52:55], a[248:251], v[188:191]\n"
    "ds_read_b64_tr_b16 a[146:147], v45 offset:512\n"
    "buffer_load_dwordx4 v18, s[48:51], 0 offen offset:1024 lds\n"
    "v_mfma_f32_16x16x32_bf16 v[188:191], a[44:47], a[244:247], v[188:191]\n"
    "s_add_u32 m0, s57, 90112\n"
    "ds_read_b128 a[216:219], v23 offset:32768\n"
    "v_add_u32_dpp v19, v10, v4 row_newbcast:14 row_mask:0x5 bank_mask:0x3\n"
    "v_mfma_f32_16x16x32_bf16 v[188:191], a[36:39], a[240:243], v[188:191]\n"
    "ds_read_b64_tr_b16 a[152:153], v45 offset:16384\n"
    "v_add_u32_dpp v19, v10, v4 row_newbcast:14 row_mask:0xa bank_mask:0xc\n"
    "v_mfma_f32_16x16x32_bf16 v[196:199], a[36:39], v[232:235], v[196:199]\n"
    "ds_read_b64_tr_b16 a[154:155], v45 offset:16896\n"
    "v_add_u32_dpp v19, v12, v4 row_newbcast:14 row_mask:0x5 bank_mask:0xc\n"
    "v_mfma_f32_16x16x32_bf16 v[196:199], a[44:47], v[236:239], v[196:199]\n"
    "ds_read_b128 a[220:223], v24 offset:32768\n"
    "v_add_u32_dpp v19, v12, v4 row_newbcast:14 row_mask:0xa bank_mask:0x3\n"
    "v_mfma_f32_16x16x32_bf16 v[196:199], a[52:55], v[240:243], v[196:199]\n"
    "ds_read_b64_tr_b16 a[156:157], v46 offset:16384\n"
    "buffer_load_dwordx4 v19, s[48:51], 0 offen offset:0 lds\n"
    "v_mfma_f32_16x16x32_bf16 v[196:199], a[60:63], v[244:247], v[196:199]\n"
    "ds_read_b64_tr_b16 a[158:159], v46 offset:16896\n"
    "v_add_u32_dpp v20, v14, v5 row_newbcast:14 row_mask:0x5 bank_mask:0x3\n"
    "v_mfma_f32_16x16x32_bf16 v[192:195], a[56:59], v[244:247], v[192:195]\n"
    "ds_read_b64_tr_b16 a[148:149], v46 offset:0\n"
    "v_add_u32_dpp v20, v14, v5 row_newbcast:14 row_mask:0xa bank_mask:0xc\n"
    "v_mfma_f32_16x16x32_bf16 v[192:195], a[48:51], v[240:243], v[192:195]\n"
    "ds_read_b64_tr_b16 a[150:151], v46 offset:512\n"
    "v_add_u32_dpp v20, v16, v5 row_newbcast:14 row_mask:0x5 bank_mask:0xc\n"
    "v_mfma_f32_16x16x32_bf16 v[192:195], a[40:43], v[236:239], v[192:195]\n"
    "ds_read_b64_tr_b16 a[140:141], v50 offset:24576\n"
    "v_add_u32_dpp v20, v16, v5 row_newbcast:14 row_mask:0xa bank_mask:0x3\n"
    "v_mfma_f32_16x16x32_bf16 v[192:195], a[32:35], v[232:235], v[192:195]\n"
    "s_waitcnt lgkmcnt(6)\n"
    "ds_read_b64_tr_b16 a[142:143], v50 offset:25088\n"
    "buffer_load_dwordx4 v20, s[48:51], 0 offen offset:1024 lds\n"
    "v_mfma_f32_16x16x32_bf16 v[104:107], a[128:131], a[208:211], v[104:107]\n"
    "s_add_u32 m0, s57, 98304\n"
    "ds_read_b128 a[224:227], v21 offset:33792\n"
    "v_add_u32_dpp v17, v12, v2 row_newbcast:2 row_mask:0xf bank_mask:0xf\n"
    "v_mfma_f32_16x16x32_bf16 v[104:107], a[136:139], a[212:215], v[104:107]\n"
    "ds_read_b128 a[228:231], v22 offset:33792\n"
    "buffer_load_dwordx4 v17, s[48:51], 0 offen offset:0 lds\n"
    "v_mfma_f32_16x16x32_bf16 v[104:107], a[144:147], a[216:219], v[104:107]\n"
    "s_waitcnt lgkmcnt(6)\n"
    "ds_read_b128 a[232:235], v23 offset:33792\n"
    "v_add_u32_dpp v18, v12, v3 row_newbcast:10 row_mask:0xf bank_mask:0xf\n"
    "v_mfma_f32_16x16x32_bf16 v[104:107], a[152:155], a[220:223], v[104:107]\n"
    "ds_read_b128 a[236:239], v24 offset:33792\n"
    "buffer_load_dwordx4 v18, s[48:51], 0 offen offset:1024 lds\n"
    "v_mfma_f32_16x16x32_bf16 v[108:111], a[156:159], a[220:223], v[108:111]\n"
    "s_waitcnt lgkmcnt(6)\n"
    "ds_read_b128 a[240:243], v21 offset:8192\n"
    "s_add_u32 m0, s57, 106496\n"
    "v_mfma_f32_16x16x32_bf16 v[108:111], a[148:151], a[216:219], v[108:111]\n"
    "s_waitcnt lgkmcnt(5)\n"
    "ds_read_b128 a[244:247], v22 offset:8192\n"
    "v_add_u32_dpp v19, v11, v2 row_newbcast:3 row_mask:0xf bank_mask:0xf\n"
    "v_mfma_f32_16x16x32_bf16 v[108:111], a[140:143], a[212:215], v[108:111]\n"
    "ds_read_b128 a[248:251], v23 offset:8192\n"
    "buffer_load_dwordx4 v19, s[48:51], 0 offen offset:0 lds\n"
    "v_mfma_f32_16x16x32_bf16 v[108:111], a[132:135], a[208:211], v[108:111]\n"
    "s_waitcnt lgkmcnt(6)\n"
    "ds_read_b128 a[252:255], v24 offset:8192\n"
    "v_add_u32_dpp v20, v11, v3 row_newbcast:11 row_mask:0xf bank_mask:0xf\n"
    "v_mfma_f32_16x16x32_bf16 v[116:119], a[132:135], a[224:227], v[116:119]\n"
    "s_waitcnt lgkmcnt(6)\n"
    "ds_read_b128 v[232:235], v21 offset:9216\n"
    "v_mfma_f32_16x16x32_bf16 v[116:119], a[140:143], a[228:231], v[116:119]\n"
    "s_waitcnt lgkmcnt(6)\n"
    "ds_read_b128 v[236:239], v22 offset:9216\n"
    "v_mfma_f32_16x16x32_bf16 v[116:119], a[148:151], a[232:235], v[116:119]\n"
    "s_waitcnt lgkmcnt(6)\n"
    "ds_read_b128 v[240:243], v23 offset:9216\n"
    "buffer_load_dwordx4 v20, s[48:51], 0 offen offset:1024 lds\n"
    "v_mfma_f32_16x16x32_bf16 v[116:119], a[156:159], a[236:239], v[116:119]\n"
    "s_add_u32 m0, s57, 114688\n"
    "ds_read_b128 v[244:247], v24 offset:9216\n"
    "v_add_u32_dpp v17, v11, v2 row_newbcast:5 row_mask:0xf bank_mask:0xf\n"
    "v_mfma_f32_16x16x32_bf16 v[112:115], a[152:155], a[236:239], v[112:115]\n"
    "v_mfma_f32_16x16x32_bf16 v[112:115], a[144:147], a[232:235], v[112:115]\n"
    "v_mfma_f32_16x16x32_bf16 v[112:115], a[136:139], a[228:231], v[112:115]\n"
    "buffer_load_dwordx4 v17, s[48:51], 0 offen offset:0 lds\n"
    "v_add_u32_dpp v18, v11, v3 row_newbcast:13 row_mask:0xf bank_mask:0xf\n"
    "v_mfma_f32_16x16x32_bf16 v[112:115], a[128:131], a[224:227], v[112:115]\n"
    "s_waitcnt vmcnt(13) lgkmcnt(0)\n"
    "s_barrier\n"
    "v_mfma_f32_16x16x32_bf16 v[120:123], a[128:131], a[240:243], v[120:123]\n"
    "ds_read_b128 a[208:211], v21 offset:24576\n"
    "buffer_load_dwordx4 v18, s[48:51], 0 offen offset:1024 lds\n"
    "v_mfma_f32_16x16x32_bf16 v[120:123], a[136:139], a[244:247], v[120:123]\n"
    "s_add_u32 m0, s57, 122880\n"
    "ds_read_b128 a[212:215], v22 offset:24576\n"
    "v_add_u32_dpp v19, v12, v2 row_newbcast:5 row_mask:0xf bank_mask:0xf\n"
    "v_mfma_f32_16x16x32_bf16 v[120:123], a[144:147], a[248:251], v[120:123]\n"
    "ds_read_b128 a[216:219], v23 offset:24576\n"
    "v_mfma_f32_16x16x32_bf16 v[120:123], a[152:155], a[252:255], v[120:123]\n"
    "ds_read_b128 a[220:223], v24 offset:24576\n"
    "v_mfma_f32_16x16x32_bf16 v[124:127], a[156:159], a[252:255], v[124:127]\n"
    "ds_read_b128 a[224:227], v21 offset:25600\n"
    "buffer_load_dwordx4 v19, s[48:51], 0 offen offset:0 lds\n"
    "v_mfma_f32_16x16x32_bf16 v[124:127], a[148:151], a[248:251], v[124:127]\n"
    "ds_read_b128 a[228:231], v22 offset:25600\n"
    "v_add_u32_dpp v20, v12, v3 row_newbcast:13 row_mask:0xf bank_mask:0xf\n"
    "v_mfma_f32_16x16x32_bf16 v[124:127], a[140:143], a[244:247], v[124:127]\n"
    "ds_read_b128 a[232:235], v23 offset:25600\n"
    "v_mfma_f32_16x16x32_bf16 v[124:127], a[132:135], a[240:243], v[124:127]\n"
    "ds_read_b128 a[236:239], v24 offset:25600\n"
    "v_mfma_f32_16x16x32_bf16 v[132:135], a[132:135], v[232:235], v[132:135]\n"
    "ds_read_b128 a[240:243], v21 offset:40960\n"
    "v_mfma_f32_16x16x32_bf16 v[132:135], a[140:143], v[236:239], v[132:135]\n"
    "ds_read_b128 a[244:247], v22 offset:40960\n"
    "buffer_load_dwordx4 v20, s[48:51], 0 offen offset:1024 lds\n"
    "v_mfma_f32_16x16x32_bf16 v[132:135], a[148:151], v[240:243], v[132:135]\n"
    "s_add_u32 m0, s57, 131072\n"
    "ds_read_b128 a[248:251], v23 offset:40960\n"
    "v_add_u32_dpp v17, v15, v2 row_newbcast:0 row_mask:0xf bank_mask:0xf\n"
    "v_mfma_f32_16x16x32_bf16 v[132:135], a[156:159], v[244:247], v[132:135]\n"
    "ds_read_b128 a[252:255], v24 offset:40960\n"
    "v_mfma_f32_16x16x32_bf16 v[128:131], a[152:155], v[244:247], v[128:131]\n"
    "ds_read_b64_tr_b16 a[192:193], v45 offset:49152\n"
    "v_mfma_f32_16x16x32_bf16 v[128:131], a[144:147], v[240:243], v[128:131]\n"
    "ds_read_b64_tr_b16 a[194:195], v45 offset:49664\n"
    "buffer_load_dwordx4 v17, s[48:51], 0 offen offset:0 lds\n"
    "v_mfma_f32_16x16x32_bf16 v[128:131], a[136:139], v[236:239], v[128:131]\n"
    "ds_read_b64_tr_b16 a[200:201], v45 offset:57344\n"
    "v_add_u32_dpp v18, v15, v3 row_newbcast:8 row_mask:0xf bank_mask:0xf\n"
    "v_mfma_f32_16x16x32_bf16 v[128:131], a[128:131], v[232:235], v[128:131]\n"
    "s_waitcnt lgkmcnt(6)\n"
    "ds_read_b128 v[232:235], v21 offset:41984\n"
    "v_mfma_f32_16x16x32_bf16 v[136:139], a[64:67], a[208:211], v[136:139]\n"
    "ds_read_b128 v[236:239], v22 offset:41984\n"
    "v_mfma_f32_16x16x32_bf16 v[136:139], a[72:75], a[212:215], v[136:139]\n"
    "ds_read_b128 v[240:243], v23 offset:41984\n"
    "buffer_load_dwordx4 v18, s[48:51], 0 offen offset:1024 lds\n"
    "v_mfma_f32_16x16x32_bf16 v[136:139], a[80:83], a[216:219], v[136:139]\n"
    "s_add_u32 m0, s57, 139264\n"
    "ds_read_b128 v[244:247], v24 offset:41984\n"
    "v_add_u32_dpp v19, v16, v2 row_newbcast:0 row_mask:0xf bank_mask:0xf\n"
    "v_mfma_f32_16x16x32_bf16 v[136:139], a[88:91], a[220:223], v[136:139]\n"
    "ds_read_b64_tr_b16 a[202:203], v45 offset:57856\n"
    "v_mfma_f32_16x16x32_bf16 v[140:143], a[92:95], a[220:223], v[140:143]\n"
    "ds_read_b64_tr_b16 a[204:205], v46 offset:57344\n"
    "v_mfma_f32_16x16x32_bf16 v[140:143], a[84:87], a[216:219], v[140:143]\n"
    "ds_read_b64_tr_b16 a[206:207], v46 offset:57856\n"
    "buffer_load_dwordx4 v19, s[48:51], 0 offen offset:0 lds\n"
    "v_mfma_f32_16x16x32_bf16 v[140:143], a[76:79], a[212:215], v[140:143]\n"
    "ds_read_b64_tr_b16 a[196:197], v46 offset:49152\n"
    "v_add_u32_dpp v20, v16, v3 row_newbcast:8 row_mask:0xf bank_mask:0xf\n"
    "v_mfma_f32_16x16x32_bf16 v[140:143], a[68:71], a[208:211], v[140:143]\n"
    "ds_read_b64_tr_b16 a[198:199], v46 offset:49664\n"
    "v_mfma_f32_16x16x32_bf16 v[148:151], a[68:71], a[224:227], v[148:151]\n"
    "v_mfma_f32_16x16x32_bf16 v[148:151], a[76:79], a[228:231], v[148:151]\n"
    "buffer_load_dwordx4 v20, s[48:51], 0 offen offset:1024 lds\n"
    "v_mfma_f32_16x16x32_bf16 v[148:151], a[84:87], a[232:235], v[148:151]\n"
    "s_add_u32 m0, s57, 147456\n"
    "v_add_u32_dpp v17, v15, v2 row_newbcast:2 row_mask:0xf bank_mask:0xf\n"
    "v_mfma_f32_16x16x32_bf16 v[148:151], a[92:95], a[236:239], v[148:151]\n"
    "v_mfma_f32_16x16x32_bf16 v[144:147], a[88:91], a[236:239], v[144:147]\n"
    "v_mfma_f32_16x16x32_bf16 v[144:147], a[80:83], a[232:235], v[144:147]\n"
    "buffer_load_dwordx4 v17, s[48:51], 0 offen offset:0 lds\n"
    "v_add_u32_dpp v18, v15, v3 row_newbcast:10 row_mask:0xf bank_mask:0xf\n"
    "v_mfma_f32_16x16x32_bf16 v[144:147], a[72:75], a[228:231], v[144:147]\n"
    "v_mfma_f32_16x16x32_bf16 v[144:147], a[64:67], a[224:227], v[144:147]\n"
    "s_waitcnt vmcnt(17) lgkmcnt(0)\n"
    "s_barrier\n"
    "v_mfma_f32_16x16x32_bf16 v[152:155], a[96:99], a[240:243], v[152:155]\n"
    "ds_read_b128 a[208:211], v37 offset:8192\n"
    "buffer_load_dwordx4 v18, s[48:51], 0 offen offset:1024 lds\n"
    "v_mfma_f32_16x16x32_bf16 v[152:155], a[104:107], a[244:247], v[152:155]\n"
    "ds_read_b128 a[212:215], v38 offset:8192\n"
    "v_and_b32 v55, 28, v55\n"
    "v_mfma_f32_16x16x32_bf16 v[152:155], a[112:115], a[248:251], v[152:155]\n"
    "ds_read_b128 a[216:219], v39 offset:8192\n"
    "v_mfma_f32_16x16x32_bf16 v[152:155], a[120:123], a[252:255], v[152:155]\n"
    "ds_read_b128 a[220:223], v40 offset:8192\n"
    "buffer_load_dword v56, v55, s[52:55], s39 offen offset:128 sc1\n"
    "v_mfma_f32_16x16x32_bf16 v[156:159], a[124:127], a[252:255], v[156:159]\n"
    "ds_read_b128 a[224:227], v37 offset:9216\n"
    "global_load_dwordx2 v[76:77], v65, s[20:21] offset:0\n"
    "v_mfma_f32_16x16x32_bf16 v[156:159], a[116:119], a[248:251], v[156:159]\n"
    "ds_read_b128 a[228:231], v38 offset:9216\n"
    "global_load_dwordx2 v[78:79], v65, s[20:21] offset:32\n"
    "v_mfma_f32_16x16x32_bf16 v[156:159], a[108:111], a[244:247], v[156:159]\n"
    "ds_read_b128 a[232:235], v39 offset:9216\n"
    "global_load_dwordx2 v[80:81], v65, s[22:23] offset:0\n"
    "v_mfma_f32_16x16x32_bf16 v[156:159], a[100:103], a[240:243], v[156:159]\n"
    "ds_read_b128 a[236:239], v40 offset:9216\n"
    "global_load_dwordx2 v[82:83], v65, s[22:23] offset:32\n"
    "v_mfma_f32_16x16x32_bf16 v[164:167], a[100:103], v[232:235], v[164:167]\n"
    "ds_read_b128 a[240:243], v37 offset:16384\n"
    "global_load_ushort v62, v64, s[24:25]\n"
    "v_mfma_f32_16x16x32_bf16 v[164:167], a[108:111], v[236:239], v[164:167]\n"
    "ds_read_b128 a[244:247], v38 offset:16384\n"
    "global_load_ushort v63, v58, s[26:27]\n"
    "v_mfma_f32_16x16x32_bf16 v[164:167], a[116:119], v[240:243], v[164:167]\n"
    "ds_read_b128 a[248:251], v39 offset:16384\n"
    "v_mfma_f32_16x16x32_bf16 v[164:167], a[124:127], v[244:247], v[164:167]\n"
    "ds_read_b128 a[252:255], v40 offset:16384\n"
    "v_mfma_f32_16x16x32_bf16 v[160:163], a[120:123], v[244:247], v[160:163]\n"
    "v_mfma_f32_16x16x32_bf16 v[160:163], a[112:115], v[240:243], v[160:163]\n"
    "v_mfma_f32_16x16x32_bf16 v[160:163], a[104:107], v[236:239], v[160:163]\n"
    "v_mfma_f32_16x16x32_bf16 v[160:163], a[96:99], v[232:235], v[160:163]\n"
    "s_waitcnt lgkmcnt(3)\n"
    "ds_read_b128 v[232:235], v37 offset:17408\n"
    "v_mfma_f32_16x16x32_bf16 v[104:107], a[192:195], a[208:211], v[104:107]\n"
    "ds_read_b128 v[236:239], v38 offset:17408\n"
    "v_mfma_f32_16x16x32_bf16 v[104:107], a[200:203], a[212:215], v[104:107]\n"
    "ds_read_b128 v[240:243], v39 offset:17408\n"
    "v_mfma_f32_16x16x32_bf16 v[108:111], a[204:207], a[212:215], v[108:111]\n"
    "ds_read_b128 v[244:247], v40 offset:17408\n"
    "v_mfma_f32_16x16x32_bf16 v[108:111], a[196:199], a[208:211], v[108:111]\n"
    "v_mfma_f32_16x16x32_bf16 v[140:143], a[196:199], a[216:219], v[140:143]\n"
    "v_mfma_f32_16x16x32_bf16 v[140:143], a[204:207], a[220:223], v[140:143]\n"
    "v_mfma_f32_16x16x32_bf16 v[136:139], a[200:203], a[220:223], v[136:139]\n"
    "v_mfma_f32_16x16x32_bf16 v[136:139], a[192:195], a[216:219], v[136:139]\n"
    "v_mfma_f32_16x16x32_bf16 v[168:171], a[192:195], a[224:227], v[168:171]\n"
    "v_mfma_f32_16x16x32_bf16 v[168:171], a[200:203], a[228:231], v[168:171]\n"
    "v_mfma_f32_16x16x32_bf16 v[172:175], a[204:207], a[228:231], v[172:175]\n"
    "v_mfma_f32_16x16x32_bf16 v[172:175], a[196:199], a[224:227], v[172:175]\n"
    "v_cvt_pk_bf16_f32 v104, v104, v105\n"
    "v_cvt_pk_bf16_f32 v105, v106, v107\n"
    "v_mfma_f32_16x16x32_bf16 v[204:207], a[196:199], a[232:235], 0\n"
    "v_cvt_pk_bf16_f32 v106, v108, v109\n"
    "v_cvt_pk_bf16_f32 v107, v110, v111\n"
    "v_mfma_f32_16x16x32_bf16 v[204:207], a[204:207], a[236:239], v[204:207]\n"
    "buffer_store_dwordx4 v[104:107], v53, s[52:55], s34 offen offset:0 sc1\n"
    "v_mfma_f32_16x16x32_bf16 v[200:203], a[200:203], a[236:239], 0\n"
    "v_mfma_f32_16x16x32_bf16 v[200:203], a[192:195], a[232:235], v[200:203]\n"
    "s_waitcnt vmcnt(22) lgkmcnt(0)\n"
    "s_barrier\n"
    "v_mfma_f32_16x16x32_bf16 v[112:115], a[192:195], a[240:243], v[112:115]\n"
    "ds_read_b128 a[208:211], v37 offset:0\n"
    "v_mfma_f32_16x16x32_bf16 v[112:115], a[200:203], a[244:247], v[112:115]\n"
    "ds_read_b128 a[212:215], v38 offset:0\n"
    "v_mfma_f32_16x16x32_bf16 v[116:119], a[204:207], a[244:247], v[116:119]\n"
    "ds_read_b128 a[216:219], v39 offset:0\n"
    "v_mfma_f32_16x16x32_bf16 v[116:119], a[196:199], a[240:243], v[116:119]\n"
    "ds_read_b128 a[220:223], v40 offset:0\n"
    "v_mfma_f32_16x16x32_bf16 v[148:151], a[196:199], a[248:251], v[148:151]\n"
    "ds_read_b128 a[224:227], v37 offset:1024\n"
    "v_mfma_f32_16x16x32_bf16 v[148:151], a[204:207], a[252:255], v[148:151]\n"
    "ds_read_b128 a[228:231], v38 offset:1024\n"
    "v_mfma_f32_16x16x32_bf16 v[144:147], a[200:203], a[252:255], v[144:147]\n"
    "ds_read_b128 a[232:235], v39 offset:1024\n"
    "v_mfma_f32_16x16x32_bf16 v[144:147], a[192:195], a[248:251], v[144:147]\n"
    "ds_read_b128 a[236:239], v40 offset:1024\n"
    "v_cvt_pk_bf16_f32 v112, v112, v113\n"
    "v_mfma_f32_16x16x32_bf16 v[176:179], a[192:195], v[232:235], v[176:179]\n"
    "ds_read_b128 a[240:243], v37 offset:24576\n"
    "v_cvt_pk_bf16_f32 v113, v114, v115\n"
    "v_mfma_f32_16x16x32_bf16 v[176:179], a[200:203], v[236:239], v[176:179]\n"
    "ds_read_b128 a[244:247], v38 offset:24576\n"
    "v_cvt_pk_bf16_f32 v114, v116, v117\n"
    "v_mfma_f32_16x16x32_bf16 v[180:183], a[204:207], v[236:239], v[180:183]\n"
    "ds_read_b128 a[248:251], v39 offset:24576\n"
    "v_cvt_pk_bf16_f32 v115, v118, v119\n"
    "v_mfma_f32_16x16x32_bf16 v[180:183], a[196:199], v[232:235], v[180:183]\n"
    "buffer_store_dwordx4 v[112:115], v53, s[52:55], s34 offen offset:1024 sc1\n"
    "v_mfma_f32_16x16x32_bf16 v[212:215], a[196:199], v[240:243], 0\n"
    "ds_read_b128 a[252:255], v40 offset:24576\n"
    "v_mfma_f32_16x16x32_bf16 v[212:215], a[204:207], v[244:247], v[212:215]\n"
    "v_mfma_f32_16x16x32_bf16 v[208:211], a[200:203], v[244:247], 0\n"
    "v_mfma_f32_16x16x32_bf16 v[208:211], a[192:195], v[240:243], v[208:211]\n"
    "s_waitcnt lgkmcnt(3)\n"
    "ds_read_b128 v[232:235], v37 offset:25600\n"
    "v_mfma_f32_16x16x32_bf16 v[120:123], a[192:195], a[208:211], v[120:123]\n"
    "ds_read_b128 v[236:239], v38 offset:25600\n"
    "v_mfma_f32_16x16x32_bf16 v[120:123], a[200:203], a[212:215], v[120:123]\n"
    "ds_read_b128 v[240:243], v39 offset:25600\n"
    "v_mfma_f32_16x16x32_bf16 v[124:127], a[204:207], a[212:215], v[124:127]\n"
    "ds_read_b128 v[244:247], v40 offset:25600\n"
    "v_mfma_f32_16x16x32_bf16 v[124:127], a[196:199], a[208:211], v[124:127]\n"
    "v_mfma_f32_16x16x32_bf16 v[156:159], a[196:199], a[216:219], v[156:159]\n"
    "v_mfma_f32_16x16x32_bf16 v[156:159], a[204:207], a[220:223], v[156:159]\n"
    "v_mfma_f32_16x16x32_bf16 v[152:155], a[200:203], a[220:223], v[152:155]\n"
    "v_mfma_f32_16x16x32_bf16 v[152:155], a[192:195], a[216:219], v[152:155]\n"
    "v_mfma_f32_16x16x32_bf16 v[184:187], a[192:195], a[224:227], v[184:187]\n"
    "v_mfma_f32_16x16x32_bf16 v[184:187], a[200:203], a[228:231], v[184:187]\n"
    "v_mfma_f32_16x16x32_bf16 v[188:191], a[204:207], a[228:231], v[188:191]\n"
    "v_mfma_f32_16x16x32_bf16 v[188:191], a[196:199], a[224:227], v[188:191]\n"
    "s_add_u32 m0, s57, 155648\n"
    "v_add_u32_dpp v19, v12, v2 row_newbcast:4 row_mask:0xf bank_mask:0xf\n"
    "v_cvt_pk_bf16_f32 v120, v120, v121\n"
    "v_mfma_f32_16x16x32_bf16 v[220:223], a[196:199], a[232:235], 0\n"
    "v_cvt_pk_bf16_f32 v121, v122, v123\n"
    "v_cvt_pk_bf16_f32 v122, v124, v125\n"
    "v_mfma_f32_16x16x32_bf16 v[220:223], a[204:207], a[236:239], v[220:223]\n"
    "v_cvt_pk_bf16_f32 v123, v126, v127\n"
    "v_mfma_f32_16x16x32_bf16 v[216:219], a[200:203], a[236:239], 0\n"
    "buffer_store_dwordx4 v[120:123], v53, s[52:55], s34 offen offset:2048 sc1\n"
    "v_mfma_f32_16x16x32_bf16 v[216:219], a[192:195], a[232:235], v[216:219]\n"
    "s_waitcnt vmcnt(20) lgkmcnt(0)\n"
    "s_barrier\n"
    "v_mfma_f32_16x16x32_bf16 v[128:131], a[192:195], a[240:243], v[128:131]\n"
    "ds_read_b128 a[208:211], v25 offset:32768\n"
    "v_mfma_f32_16x16x32_bf16 v[128:131], a[200:203], a[244:247], v[128:131]\n"
    "ds_read_b128 a[212:215], v26 offset:32768\n"
    "v_mfma_f32_16x16x32_bf16 v[132:135], a[204:207], a[244:247], v[132:135]\n"
    "ds_read_b128 a[216:219], v27 offset:32768\n"
    "v_mfma_f32_16x16x32_bf16 v[132:135], a[196:199], a[240:243], v[132:135]\n"
    "ds_read_b128 a[220:223], v28 offset:32768\n"
    "v_mfma_f32_16x16x32_bf16 v[164:167], a[196:199], a[248:251], v[164:167]\n"
    "ds_read_b128 a[224:227], v25 offset:33792\n"
    "v_mfma_f32_16x16x32_bf16 v[164:167], a[204:207], a[252:255], v[164:167]\n"
    "ds_read_b128 a[228:231], v26 offset:33792\n"
    "v_mfma_f32_16x16x32_bf16 v[160:163], a[200:203], a[252:255], v[160:163]\n"
    "ds_read_b128 a[232:235], v27 offset:33792\n"
    "v_mfma_f32_16x16x32_bf16 v[160:163], a[192:195], a[248:251], v[160:163]\n"
    "ds_read_b128 a[236:239], v28 offset:33792\n"
    "v_cvt_pk_bf16_f32 v128, v128, v129\n"
    "v_mfma_f32_16x16x32_bf16 v[192:195], a[192:195], v[232:235], v[192:195]\n"
    "ds_read_b128 a[240:243], v25 offset:40960\n"
    "v_cvt_pk_bf16_f32 v129, v130, v131\n"
    "v_mfma_f32_16x16x32_bf16 v[192:195], a[200:203], v[236:239], v[192:195]\n"
    "ds_read_b128 a[244:247], v26 offset:40960\n"
    "v_cvt_pk_bf16_f32 v130, v132, v133\n"
    "v_mfma_f32_16x16x32_bf16 v[196:199], a[204:207], v[236:239], v[196:199]\n"
    "ds_read_b128 a[248:251], v27 offset:40960\n"
    "v_cvt_pk_bf16_f32 v131, v134, v135\n"
    "v_mfma_f32_16x16x32_bf16 v[196:199], a[196:199], v[232:235], v[196:199]\n"
    "ds_read_b128 a[252:255], v28 offset:40960\n"
    "v_mfma_f32_16x16x32_bf16 v[228:231], a[196:199], v[240:243], 0\n"
    "buffer_store_dwordx4 v[128:131], v53, s[52:55], s34 offen offset:3072 sc1\n"
    "v_mfma_f32_16x16x32_bf16 v[228:231], a[204:207], v[244:247], v[228:231]\n"
    "v_mfma_f32_16x16x32_bf16 v[224:227], a[200:203], v[244:247], 0\n"
    "v_mfma_f32_16x16x32_bf16 v[224:227], a[192:195], v[240:243], v[224:227]\n"
    "s_waitcnt lgkmcnt(3)\n"
    "ds_read_b128 v[232:235], v25 offset:41984\n"
    "v_mfma_f32_16x16x32_bf16 v[152:155], a[64:67], a[208:211], v[152:155]\n"
    "ds_read_b128 v[236:239], v26 offset:41984\n"
    "v_mfma_f32_16x16x32_bf16 v[152:155], a[72:75], a[212:215], v[152:155]\n"
    "ds_read_b128 v[240:243], v27 offset:41984\n"
    "v_mfma_f32_16x16x32_bf16 v[152:155], a[80:83], a[216:219], v[152:155]\n"
    "ds_read_b128 v[244:247], v28 offset:41984\n"
    "v_mfma_f32_16x16x32_bf16 v[152:155], a[88:91], a[220:223], v[152:155]\n"
    "v_mfma_f32_16x16x32_bf16 v[156:159], a[92:95], a[220:223], v[156:159]\n"
    "v_mfma_f32_16x16x32_bf16 v[156:159], a[84:87], a[216:219], v[156:159]\n"
    "v_mfma_f32_16x16x32_bf16 v[156:159], a[76:79], a[212:215], v[156:159]\n"
    "v_mfma_f32_16x16x32_bf16 v[156:159], a[68:71], a[208:211], v[156:159]\n"
    "v_mfma_f32_16x16x32_bf16 v[164:167], a[68:71], a[224:227], v[164:167]\n"
    "v_mfma_f32_16x16x32_bf16 v[164:167], a[76:79], a[228:231], v[164:167]\n"
    "v_mfma_f32_16x16x32_bf16 v[164:167], a[84:87], a[232:235], v[164:167]\n"
    "v_mfma_f32_16x16x32_bf16 v[164:167], a[92:95], a[236:239], v[164:167]\n"
    "v_mfma_f32_16x16x32_bf16 v[160:163], a[88:91], a[236:239], v[160:163]\n"
    "v_mfma_f32_16x16x32_bf16 v[160:163], a[80:83], a[232:235], v[160:163]\n"
    "v_mfma_f32_16x16x32_bf16 v[160:163], a[72:75], a[228:231], v[160:163]\n"
    "v_mfma_f32_16x16x32_bf16 v[160:163], a[64:67], a[224:227], v[160:163]\n"
    "buffer_load_dwordx4 v19, s[48:51], 0 offen offset:0 lds\n"
    "s_waitcnt vmcnt(18) lgkmcnt(0)\n"
    "s_barrier\n"
    "v_mfma_f32_16x16x32_bf16 v[136:139], a[96:99], a[240:243], v[136:139]\n"
    "ds_read_b128 a[208:211], v25 offset:49152\n"
    "v_add_u32_dpp v20, v12, v3 row_newbcast:12 row_mask:0xf bank_mask:0xf\n"
    "v_mfma_f32_16x16x32_bf16 v[136:139], a[104:107], a[244:247], v[136:139]\n"
    "ds_read_b128 a[212:215], v26 offset:49152\n"
    "buffer_load_dwordx4 v20, s[48:51], 0 offen offset:1024 lds\n"
    "v_mfma_f32_16x16x32_bf16 v[136:139], a[112:115], a[248:251], v[136:139]\n"
    "s_add_u32 m0, s57, 0\n"
    "ds_read_b128 a[216:219], v27 offset:49152\n"
    "v_add_u32_dpp v17, v11, v2 row_newbcast:4 row_mask:0xf bank_mask:0xf\n"
    "v_mfma_f32_16x16x32_bf16 v[136:139], a[120:123], a[252:255], v[136:139]\n"
    "ds_read_b128 a[220:223], v28 offset:49152\n"
    "buffer_load_dwordx4 v17, s[48:51], 0 offen offset:0 lds\n"
    "v_mfma_f32_16x16x32_bf16 v[140:143], a[124:127], a[252:255], v[140:143]\n"
    "ds_read_b128 a[224:227], v25 offset:50176\n"
    "v_add_u32_dpp v18, v11, v3 row_newbcast:12 row_mask:0xf bank_mask:0xf\n"
    "v_mfma_f32_16x16x32_bf16 v[140:143], a[116:119], a[248:251], v[140:143]\n"
    "ds_read_b128 a[228:231], v26 offset:50176\n"
    "buffer_load_dwordx4 v18, s[48:51], 0 offen offset:1024 lds\n"
    "v_mfma_f32_16x16x32_bf16 v[140:143], a[108:111], a[244:247], v[140:143]\n"
    "s_add_u32 m0, s57, 16384\n"
    "ds_read_b128 a[232:235], v27 offset:50176\n"
    "v_add_u32_dpp v19, v13, v2 row_newbcast:3 row_mask:0xf bank_mask:0xf\n"
    "v_mfma_f32_16x16x32_bf16 v[140:143], a[100:103], a[240:243], v[140:143]\n"
    "ds_read_b128 a[236:239], v28 offset:50176\n"
    "buffer_load_dwordx4 v19, s[48:51], 0 offen offset:0 lds\n"
    "v_mfma_f32_16x16x32_bf16 v[148:151], a[100:103], v[232:235], v[148:151]\n"
    "ds_read_b128 a[240:243], v25 offset:57344\n"
    "v_add_u32_dpp v20, v13, v3 row_newbcast:11 row_mask:0xf bank_mask:0xf\n"
    "v_mfma_f32_16x16x32_bf16 v[148:151], a[108:111], v[236:239], v[148:151]\n"
    "ds_read_b128 a[244:247], v26 offset:57344\n"
    "v_mfma_f32_16x16x32_bf16 v[148:151], a[116:119], v[240:243], v[148:151]\n"
    "ds_read_b128 a[248:251], v27 offset:57344\n"
    "v_mfma_f32_16x16x32_bf16 v[148:151], a[124:127], v[244:247], v[148:151]\n"
    "ds_read_b128 a[252:255], v28 offset:57344\n"
    "v_mfma_f32_16x16x32_bf16 v[144:147], a[120:123], v[244:247], v[144:147]\n"
    "v_mfma_f32_16x16x32_bf16 v[144:147], a[112:115], v[240:243], v[144:147]\n"
    "v_mfma_f32_16x16x32_bf16 v[144:147], a[104:107], v[236:239], v[144:147]\n"
    "v_mfma_f32_16x16x32_bf16 v[144:147], a[96:99], v[232:235], v[144:147]\n"
    "s_waitcnt lgkmcnt(3)\n"
    "ds_read_b128 v[232:235], v25 offset:58368\n"
    "v_mfma_f32_16x16x32_bf16 v[136:139], a[160:163], a[208:211], v[136:139]\n"
    "ds_read_b128 v[236:239], v26 offset:58368\n"
    "v_mfma_f32_16x16x32_bf16 v[136:139], a[168:171], a[212:215], v[136:139]\n"
    "ds_read_b128 v[240:243], v27 offset:58368\n"
    "buffer_load_dwordx4 v20, s[48:51], 0 offen offset:1024 lds\n"
    "v_mfma_f32_16x16x32_bf16 v[136:139], a[176:179], a[216:219], v[136:139]\n"
    "s_add_u32 m0, s57, 32768\n"
    "ds_read_b128 v[244:247], v28 offset:58368\n"
    "v_add_u32_dpp v17, v13, v2 row_newbcast:5 row_mask:0xf bank_mask:0xf\n"
    "v_mfma_f32_16x16x32_bf16 v[136:139], a[184:187], a[220:223], v[136:139]\n"
    "v_mfma_f32_16x16x32_bf16 v[140:143], a[188:191], a[220:223], v[140:143]\n"
    "buffer_load_dwordx4 v17, s[48:51], 0 offen offset:0 lds\n"
    "v_add_u32_dpp v18, v13, v3 row_newbcast:13 row_mask:0xf bank_mask:0xf\n"
    "v_mfma_f32_16x16x32_bf16 v[140:143], a[180:183], a[216:219], v[140:143]\n"
    "v_mfma_f32_16x16x32_bf16 v[140:143], a[172:175], a[212:215], v[140:143]\n"
    "v_mfma_f32_16x16x32_bf16 v[140:143], a[164:167], a[208:211], v[140:143]\n"
    "v_mfma_f32_16x16x32_bf16 v[148:151], a[164:167], a[224:227], v[148:151]\n"
    "v_mfma_f32_16x16x32_bf16 v[148:151], a[172:175], a[228:231], v[148:151]\n"
    "v_mfma_f32_16x16x32_bf16 v[148:151], a[180:183], a[232:235], v[148:151]\n"
    "v_mfma_f32_16x16x32_bf16 v[148:151], a[188:191], a[236:239], v[148:151]\n"
    "v_mfma_f32_16x16x32_bf16 v[144:147], a[184:187], a[236:239], v[144:147]\n"
    "v_mfma_f32_16x16x32_bf16 v[144:147], a[176:179], a[232:235], v[144:147]\n"
    "buffer_load_dwordx4 v18, s[48:51], 0 offen offset:1024 lds\n"
    "v_mfma_f32_16x16x32_bf16 v[144:147], a[168:171], a[228:231], v[144:147]\n"
    "s_add_u32 m0, s57, 8192\n"
    "v_add_u32_dpp v19, v13, v2 row_newbcast:4 row_mask:0xf bank_mask:0xf\n"
    "v_mfma_f32_16x16x32_bf16 v[144:147], a[160:163], a[224:227], v[144:147]\n"
    "buffer_load_dwordx4 v19, s[48:51], 0 offen offset:0 lds\n"
    "v_add_u32_dpp v20, v13, v3 row_newbcast:12 row_mask:0xf bank_mask:0xf\n"
    "buffer_load_dwordx4 v20, s[48:51], 0 offen offset:1024 lds\n"
    "s_add_u32 m0, s57, 24576\n"
    "v_add_u32_dpp v17, v13, v2 row_newbcast:2 row_mask:0xf bank_mask:0xf\n"
    "buffer_load_dwordx4 v17, s[48:51], 0 offen offset:0 lds\n"
    "v_add_u32_dpp v18, v13, v3 row_newbcast:10 row_mask:0xf bank_mask:0xf\n"
    "buffer_load_dwordx4 v18, s[48:51], 0 offen offset:1024 lds\n"
    "s_waitcnt vmcnt(8) lgkmcnt(0)\n"
    "s_barrier\n"
    "v_mfma_f32_16x16x32_bf16 v[152:155], a[160:163], a[240:243], v[152:155]\n"
    "ds_read_b128 a[208:211], v29 offset:24576\n"
    "v_cmp_ne_u32 vcc, 1, v56\n"
    "v_mfma_f32_16x16x32_bf16 v[152:155], a[168:171], a[244:247], v[152:155]\n"
    "s_cbranch_vccnz .Lwr_%=\n"
    ".Lokr_%=:\n"
    "ds_read_b128 a[212:215], v30 offset:24576\n"
    "buffer_store_dword v57, v54, s[52:55], 0 offen sc1\n"
    "v_mfma_f32_16x16x32_bf16 v[152:155], a[176:179], a[248:251], v[152:155]\n"
    "ds_read_b128 a[216:219], v31 offset:24576\n"
    "s_xor_b32 s62, s59, 256\n"
    "v_mfma_f32_16x16x32_bf16 v[152:155], a[184:187], a[252:255], v[152:155]\n"
    "s_add_u32 m0, s57, 40960\n"
    "ds_read_b128 a[220:223], v32 offset:24576\n"
    "buffer_store_dword v57, off, s[52:55], s62 offset:192 sc1\n"
    "v_mfma_f32_16x16x32_bf16 v[156:159], a[188:191], a[252:255], v[156:159]\n"
    "ds_read_b128 a[224:227], v29 offset:25600\n"
    "v_add_u32_dpp v19, v14, v2 row_newbcast:3 row_mask:0xf bank_mask:0xf\n"
    "v_mfma_f32_16x16x32_bf16 v[156:159], a[180:183], a[248:251], v[156:159]\n"
    "ds_read_b128 a[228:231], v30 offset:25600\n"
    "buffer_load_dwordx4 v19, s[48:51], 0 offen offset:0 lds\n"
    "v_mfma_f32_16x16x32_bf16 v[156:159], a[172:175], a[244:247], v[156:159]\n"
    "ds_read_b128 a[232:235], v31 offset:25600\n"
    "v_add_u32_dpp v20, v14, v3 row_newbcast:11 row_mask:0xf bank_mask:0xf\n"
    "v_mfma_f32_16x16x32_bf16 v[156:159], a[164:167], a[240:243], v[156:159]\n"
    "ds_read_b128 a[236:239], v32 offset:25600\n"
    "buffer_load_dwordx4 v20, s[48:51], 0 offen offset:1024 lds\n"
    "v_mfma_f32_16x16x32_bf16 v[164:167], a[164:167], v[232:235], v[164:167]\n"
    "s_add_u32 m0, s57, 49152\n"
    "ds_read_b128 a[240:243], v21 offset:0\n"
    "v_add_u32_dpp v17, v14, v2 row_newbcast:2 row_mask:0xf bank_mask:0xf\n"
    "v_mfma_f32_16x16x32_bf16 v[164:167], a[172:175], v[236:239], v[164:167]\n"
    "ds_read_b128 a[244:247], v22 offset:0\n"
    "buffer_load_dwordx4 v17, s[48:51], 0 offen offset:0 lds\n"
    "v_mfma_f32_16x16x32_bf16 v[164:167], a[180:183], v[240:243], v[164:167]\n"
    "ds_read_b128 a[248:251], v23 offset:0\n"
    "v_add_u32_dpp v18, v14, v3 row_newbcast:10 row_mask:0xf bank_mask:0xf\n"
    "v_mfma_f32_16x16x32_bf16 v[164:167], a[188:191], v[244:247], v[164:167]\n"
    "ds_read_b128 a[252:255], v24 offset:0\n"
    "v_lshlrev_b32 v84, 16, v76\n"
    "v_mfma_f32_16x16x32_bf16 v[160:163], a[184:187], v[244:247], v[160:163]\n"
    "v_and_b32 v85, 0xffff0000, v76\n"
    "v_lshlrev_b32 v86, 16, v77\n"
    "v_mfma_f32_16x16x32_bf16 v[160:163], a[176:179], v[240:243], v[160:163]\n"
    "buffer_load_dwordx4 v18, s[48:51], 0 offen offset:1024 lds\n"
    "v_and_b32 v87, 0xffff0000, v77\n"
    "v_mfma_f32_16x16x32_bf16 v[160:163], a[168:171], v[236:239], v[160:163]\n"
    "s_add_u32 m0, s57, 57344\n"
    "v_add_u32_dpp v19, v14, v2 row_newbcast:5 row_mask:0xf bank_mask:0xf\n"
    "v_lshlrev_b32 v88, 16, v78\n"
    "v_mfma_f32_16x16x32_bf16 v[160:163], a[160:163], v[232:235], v[160:163]\n"
    "s_waitcnt lgkmcnt(3)\n"
    "ds_read_b128 v[232:235], v21 offset:1024\n"
    "v_and_b32 v89, 0xffff0000, v78\n"
    "v_mfma_f32_16x16x32_bf16 v[152:155], a[128:131], a[208:211], v[152:155]\n"
    "ds_read_b128 v[236:239], v22 offset:1024\n"
    "v_lshlrev_b32 v90, 16, v79\n"
    "v_mfma_f32_16x16x32_bf16 v[152:155], a[136:139], a[212:215], v[152:155]\n"
    "ds_read_b128 v[240:243], v23 offset:1024\n"
    "buffer_load_dwordx4 v19, s[48:51], 0 offen offset:0 lds\n"
    "v_mfma_f32_16x16x32_bf16 v[152:155], a[144:147], a[216:219], v[152:155]\n"
    "ds_read_b128 v[244:247], v24 offset:1024\n"
    "v_add_u32_dpp v20, v14, v3 row_newbcast:13 row_mask:0xf bank_mask:0xf\n"
    "v_mfma_f32_16x16x32_bf16 v[152:155], a[152:155], a[220:223], v[152:155]\n"
    "v_and_b32 v91, 0xffff0000, v79\n"
    "v_lshlrev_b32 v62, 16, v62\n"
    "v_mfma_f32_16x16x32_bf16 v[156:159], a[156:159], a[220:223], v[156:159]\n"
    "v_lshlrev_b32 v63, 16, v63\n"
    "v_and_b32 v62, s16, v62\n"
    "v_mfma_f32_16x16x32_bf16 v[156:159], a[148:151], a[216:219], v[156:159]\n"
    "buffer_load_dwordx4 v20, s[48:51], 0 offen offset:1024 lds\n"
    "v_and_b32 v63, s60, v63\n"
    "v_mfma_f32_16x16x32_bf16 v[156:159], a[140:143], a[212:215], v[156:159]\n"
    "s_add_u32 m0, s57, 73728\n"
    "v_add_u32_dpp v17, v14, v2 row_newbcast:4 row_mask:0xf bank_mask:0xf\n"
    "v_or_b32 v62, s17, v62\n"
    "v_mfma_f32_16x16x32_bf16 v[156:159], a[132:135], a[208:211], v[156:159]\n"
    "v_mul_f32 v100, v62, v63\n"
    "v_bfe_u32 v68, v1, 2, 2\n"
    "v_mfma_f32_16x16x32_bf16 v[164:167], a[132:135], a[224:227], v[164:167]\n"
    "v_xor_b32 v69, 0, v68\n"
    "v_subrev_u32 v69, 1, v69\n"
    "v_mfma_f32_16x16x32_bf16 v[164:167], a[140:143], a[228:231], v[164:167]\n"
    "buffer_load_dwordx4 v17, s[48:51], 0 offen offset:0 lds\n"
    "v_add_u32_dpp v18, v14, v3 row_newbcast:12 row_mask:0xf bank_mask:0xf\n"
    "v_mfma_f32_16x16x32_bf16 v[164:167], a[148:151], a[232:235], v[164:167]\n"
    "v_cvt_pk_bf16_f32 v152, v152, v153\n"
    "v_cvt_pk_bf16_f32 v153, v154, v155\n"
    "v_mfma_f32_16x16x32_bf16 v[164:167], a[156:159], a[236:239], v[164:167]\n"
    "v_cvt_pk_bf16_f32 v154, v156, v157\n"
    "v_cvt_pk_bf16_f32 v155, v158, v159\n"
    "v_mfma_f32_16x16x32_bf16 v[160:163], a[152:155], a[236:239], v[160:163]\n"
    "buffer_store_dwordx4 v[152:155], v53, s[52:55], s35 offen offset:2048 sc1\n"
    "v_mfma_f32_16x16x32_bf16 v[160:163], a[144:147], a[232:235], v[160:163]\n"
    "v_ashrrev_i32 v69, 31, v69\n"
    "v_and_b32 v104, v69, v80\n"
    "v_mfma_f32_16x16x32_bf16 v[160:163], a[136:139], a[228:231], v[160:163]\n"
    "v_and_b32 v105, v69, v81\n"
    "v_and_b32 v106, v69, v82\n"
    "v_mfma_f32_16x16x32_bf16 v[160:163], a[128:131], a[224:227], v[160:163]\n"
    "s_waitcnt vmcnt(14) lgkmcnt(0)\n"
    "s_barrier\n"
    "v_mfma_f32_16x16x32_bf16 v[136:139], a[128:131], a[240:243], v[136:139]\n"
    "ds_read_b128 a[208:211], v21 offset:16384\n"
    "v_and_b32 v107, v69, v83\n"
    "v_mfma_f32_16x16x32_bf16 v[136:139], a[136:139], a[244:247], v[136:139]\n"
    "ds_read_b128 a[212:215], v22 offset:16384\n"
    "v_xor_b32 v69, 1, v68\n"
    "v_mfma_f32_16x16x32_bf16 v[136:139], a[144:147], a[248:251], v[136:139]\n"
    "ds_read_b128 a[216:219], v23 offset:16384\n"
    "v_cvt_pk_bf16_f32 v160, v160, v161\n"
    "v_mfma_f32_16x16x32_bf16 v[136:139], a[152:155], a[252:255], v[136:139]\n"
    "ds_read_b128 a[220:223], v24 offset:16384\n"
    "v_cvt_pk_bf16_f32 v161, v162, v163\n"
    "v_mfma_f32_16x16x32_bf16 v[140:143], a[156:159], a[252:255], v[140:143]\n"
    "ds_read_b128 a[224:227], v21 offset:17408\n"
    "v_cvt_pk_bf16_f32 v162, v164, v165\n"
    "v_mfma_f32_16x16x32_bf16 v[140:143], a[148:151], a[248:251], v[140:143]\n"
    "ds_read_b128 a[228:231], v22 offset:17408\n"
    "v_cvt_pk_bf16_f32 v163, v166, v167\n"
    "v_mfma_f32_16x16x32_bf16 v[140:143], a[140:143], a[244:247], v[140:143]\n"
    "ds_read_b128 a[232:235], v23 offset:17408\n"
    "buffer_load_dwordx4 v18, s[48:51], 0 offen offset:1024 lds\n"
    "v_mfma_f32_16x16x32_bf16 v[140:143], a[132:135], a[240:243], v[140:143]\n"
    "s_add_u32 m0, s57, 81920\n"
    "ds_read_b128 a[236:239], v24 offset:17408\n"
    "v_add_u32_dpp v19, v15, v2 row_newbcast:1 row_mask:0xf bank_mask:0xf\n"
    "v_mfma_f32_16x16x32_bf16 v[148:151], a[132:135], v[232:235], v[148:151]\n"
    "ds_read_b128 a[240:243], v21 offset:32768\n"
    "buffer_load_dwordx4 v19, s[48:51], 0 offen offset:0 lds\n"
    "v_mfma_f32_16x16x32_bf16 v[148:151], a[140:143], v[236:239], v[148:151]\n"
    "ds_read_b128 a[244:247], v22 offset:32768\n"
    "v_add_u32_dpp v20, v15, v3 row_newbcast:9 row_mask:0xf bank_mask:0xf\n"
    "v_mfma_f32_16x16x32_bf16 v[148:151], a[148:151], v[240:243], v[148:151]\n"
    "buffer_store_dwordx4 v[160:163], v53, s[52:55], s35 offen offset:3072 sc1\n"
    "v_mfma_f32_16x16x32_bf16 v[148:151], a[156:159], v[244:247], v[148:151]\n"
    "ds_read_b128 a[248:251], v23 offset:32768\n"
    "v_cvt_pk_bf16_f32 v136, v136, v137\n"
    "v_mfma_f32_16x16x32_bf16 v[144:147], a[152:155], v[244:247], v[144:147]\n"
    "ds_read_b128 a[252:255], v24 offset:32768\n"
    "v_cvt_pk_bf16_f32 v137, v138, v139\n"
    "v_mfma_f32_16x16x32_bf16 v[144:147], a[144:147], v[240:243], v[144:147]\n"
    "v_cvt_pk_bf16_f32 v138, v140, v141\n"
    "v_cvt_pk_bf16_f32 v139, v142, v143\n"
    "v_mfma_f32_16x16x32_bf16 v[144:147], a[136:139], v[236:239], v[144:147]\n"
    "v_subrev_u32 v69, 1, v69\n"
    "v_ashrrev_i32 v69, 31, v69\n"
    "v_mfma_f32_16x16x32_bf16 v[144:147], a[128:131], v[232:235], v[144:147]\n"
    "s_waitcnt lgkmcnt(3)\n"
    "ds_read_b128 v[232:235], v21 offset:33792\n"
    "v_and_b32 v112, v69, v80\n"
    "v_mfma_f32_16x16x32_bf16 v[168:171], a[96:99], a[208:211], v[168:171]\n"
    "ds_read_b128 v[236:239], v22 offset:33792\n"
    "v_and_b32 v113, v69, v81\n"
    "v_mfma_f32_16x16x32_bf16 v[168:171], a[104:107], a[212:215], v[168:171]\n"
    "ds_read_b128 v[240:243], v23 offset:33792\n"
    "v_and_b32 v114, v69, v82\n"
    "v_mfma_f32_16x16x32_bf16 v[168:171], a[112:115], a[216:219], v[168:171]\n"
    "ds_read_b128 v[244:247], v24 offset:33792\n"
    "v_and_b32 v115, v69, v83\n"
    "v_mfma_f32_16x16x32_bf16 v[168:171], a[120:123], a[220:223], v[168:171]\n"
    "v_xor_b32 v69, 2, v68\n"
    "v_subrev_u32 v69, 1, v69\n"
    "v_mfma_f32_16x16x32_bf16 v[172:175], a[124:127], a[220:223], v[172:175]\n"
    "v_ashrrev_i32 v69, 31, v69\n"
    "v_and_b32 v120, v69, v80\n"
    "v_mfma_f32_16x16x32_bf16 v[172:175], a[116:119], a[216:219], v[172:175]\n"
    "v_and_b32 v121, v69, v81\n"
    "v_and_b32 v122, v69, v82\n"
    "v_mfma_f32_16x16x32_bf16 v[172:175], a[108:111], a[212:215], v[172:175]\n"
    "v_and_b32 v123, v69, v83\n"
    "v_xor_b32 v69, 3, v68\n"
    "v_mfma_f32_16x16x32_bf16 v[172:175], a[100:103], a[208:211], v[172:175]\n"
    "v_subrev_u32 v69, 1, v69\n"
    "v_ashrrev_i32 v69, 31, v69\n"
    "v_mfma_f32_16x16x32_bf16 v[180:183], a[100:103], a[224:227], v[180:183]\n"
    "v_and_b32 v128, v69, v80\n"
    "v_and_b32 v129, v69, v81\n"
    "v_mfma_f32_16x16x32_bf16 v[180:183], a[108:111], a[228:231], v[180:183]\n"
    "v_and_b32 v130, v69, v82\n"
    "v_and_b32 v131, v69, v83\n"
    "v_mfma_f32_16x16x32_bf16 v[180:183], a[116:119], a[232:235], v[180:183]\n"
    "v_mfma_f32_16x16x32_bf16 v[180:183], a[124:127], a[236:239], v[180:183]\n"
    "v_mfma_f32_16x16x32_bf16 v[176:179], a[120:123], a[236:239], v[176:179]\n"
    "buffer_store_dwordx4 v[136:139], v53, s[52:55], s35 offen offset:0 sc1\n"
    "v_mfma_f32_16x16x32_bf16 v[176:179], a[112:115], a[232:235], v[176:179]\n"
    "v_cvt_pk_bf16_f32 v144, v144, v145\n"
    "v_cvt_pk_bf16_f32 v145, v146, v147\n"
    "v_mfma_f32_16x16x32_bf16 v[176:179], a[104:107], a[228:231], v[176:179]\n"
    "v_cvt_pk_bf16_f32 v146, v148, v149\n"
    "v_cvt_pk_bf16_f32 v147, v150, v151\n"
    "v_mfma_f32_16x16x32_bf16 v[176:179], a[96:99], a[224:227], v[176:179]\n"
    "s_waitcnt vmcnt(14) lgkmcnt(0)\n"
    "s_barrier\n"
    "v_mfma_f32_16x16x32_bf16 v[168:171], a[160:163], a[240:243], v[168:171]\n"
    "ds_read_b128 a[208:211], v21 offset:8192\n"
    "v_mfma_f32_16x16x32_bf16 v[168:171], a[168:171], a[244:247], v[168:171]\n"
    "ds_read_b128 a[212:215], v22 offset:8192\n"
    "v_mfma_f32_16x16x32_bf16 v[168:171], a[176:179], a[248:251], v[168:171]\n"
    "ds_read_b128 a[216:219], v23 offset:8192\n"
    "v_mfma_f32_16x16x32_bf16 v[168:171], a[184:187], a[252:255], v[168:171]\n"
    "ds_read_b128 a[220:223], v24 offset:8192\n"
    "v_mfma_f32_16x16x32_bf16 v[172:175], a[188:191], a[252:255], v[172:175]\n"
    "ds_read_b128 a[224:227], v21 offset:9216\n"
    "v_mfma_f32_16x16x32_bf16 v[172:175], a[180:183], a[248:251], v[172:175]\n"
    "ds_read_b128 a[228:231], v22 offset:9216\n"
    "v_mfma_f32_16x16x32_bf16 v[172:175], a[172:175], a[244:247], v[172:175]\n"
    "ds_read_b128 a[232:235], v23 offset:9216\n"
    "v_mfma_f32_16x16x32_bf16 v[172:175], a[164:167], a[240:243], v[172:175]\n"
    "ds_read_b128 a[236:239], v24 offset:9216\n"
    "v_mfma_f32_16x16x32_bf16 v[180:183], a[164:167], v[232:235], v[180:183]\n"
    "ds_read_b128 a[240:243], v21 offset:24576\n"
    "v_mfma_f32_16x16x32_bf16 v[180:183], a[172:175], v[236:239], v[180:183]\n"
    "ds_read_b128 a[244:247], v22 offset:24576\n"
    "v_mfma_f32_16x16x32_bf16 v[180:183], a[180:183], v[240:243], v[180:183]\n"
    "ds_read_b128 a[248:251], v23 offset:24576\n"
    "v_mfma_f32_16x16x32_bf16 v[180:183], a[188:191], v[244:247], v[180:183]\n"
    "ds_read_b128 a[252:255], v24 offset:24576\n"
    "v_mfma_f32_16x16x32_bf16 v[176:179], a[184:187], v[244:247], v[176:179]\n"
    "v_mfma_f32_16x16x32_bf16 v[176:179], a[176:179], v[240:243], v[176:179]\n"
    "buffer_store_dwordx4 v[144:147], v53, s[52:55], s35 offen offset:1024 sc1\n"
    "v_mfma_f32_16x16x32_bf16 v[176:179], a[168:171], v[236:239], v[176:179]\n"
    "v_mfma_f32_16x16x32_bf16 v[176:179], a[160:163], v[232:235], v[176:179]\n"
    "s_waitcnt lgkmcnt(3)\n"
    "ds_read_b128 v[232:235], v21 offset:25600\n"
    "v_mfma_f32_16x16x32_bf16 v[168:171], a[128:131], a[208:211], v[168:171]\n"
    "ds_read_b128 v[236:239], v22 offset:25600\n"
    "v_mfma_f32_16x16x32_bf16 v[168:171], a[136:139], a[212:215], v[168:171]\n"
    "ds_read_b128 v[240:243], v23 offset:25600\n"
    "v_mfma_f32_16x16x32_bf16 v[168:171], a[144:147], a[216:219], v[168:171]\n"
    "ds_read_b128 v[244:247], v24 offset:25600\n"
    "v_mfma_f32_16x16x32_bf16 v[168:171], a[152:155], a[220:223], v[168:171]\n"
    "v_mfma_f32_16x16x32_bf16 v[172:175], a[156:159], a[220:223], v[172:175]\n"
    "v_mfma_f32_16x16x32_bf16 v[172:175], a[148:151], a[216:219], v[172:175]\n"
    "v_mfma_f32_16x16x32_bf16 v[172:175], a[140:143], a[212:215], v[172:175]\n"
    "v_mfma_f32_16x16x32_bf16 v[172:175], a[132:135], a[208:211], v[172:175]\n"
    "v_mfma_f32_16x16x32_bf16 v[180:183], a[132:135], a[224:227], v[180:183]\n"
    "v_mfma_f32_16x16x32_bf16 v[180:183], a[140:143], a[228:231], v[180:183]\n"
    "v_mfma_f32_16x16x32_bf16 v[180:183], a[148:151], a[232:235], v[180:183]\n"
    "v_mfma_f32_16x16x32_bf16 v[180:183], a[156:159], a[236:239], v[180:183]\n"
    "v_mfma_f32_16x16x32_bf16 v[176:179], a[152:155], a[236:239], v[176:179]\n"
    "v_mfma_f32_16x16x32_bf16 v[176:179], a[144:147], a[232:235], v[176:179]\n"
    "v_mfma_f32_16x16x32_bf16 v[176:179], a[136:139], a[228:231], v[176:179]\n"
    "v_mfma_f32_16x16x32_bf16 v[176:179], a[128:131], a[224:227], v[176:179]\n"
    "s_waitcnt vmcnt(9) lgkmcnt(0)\n"
    "s_barrier\n"
    "v_mfma_f32_16x16x32_bf16 v[168:171], a[64:67], a[240:243], v[168:171]\n"
    "ds_read_b128 a[208:211], v21 offset:40960\n"
    "buffer_load_dwordx4 v20, s[48:51], 0 offen offset:1024 lds\n"
    "v_mfma_f32_16x16x32_bf16 v[168:171], a[72:75], a[244:247], v[168:171]\n"
    "s_add_u32 m0, s57, 65536\n"
    "ds_read_b128 a[212:215], v22 offset:40960\n"
    "v_add_u32_dpp v17, v16, v2 row_newbcast:2 row_mask:0xf bank_mask:0xf\n"
    "v_mfma_f32_16x16x32_bf16 v[168:171], a[80:83], a[248:251], v[168:171]\n"
    "ds_read_b128 a[216:219], v23 offset:40960\n"
    "buffer_load_dwordx4 v17, s[48:51], 0 offen offset:0 lds\n"
    "v_mfma_f32_16x16x32_bf16 v[168:171], a[88:91], a[252:255], v[168:171]\n"
    "ds_read_b128 a[220:223], v24 offset:40960\n"
    "v_add_u32_dpp v18, v16, v3 row_newbcast:10 row_mask:0xf bank_mask:0xf\n"
    "v_mfma_f32_16x16x32_bf16 v[172:175], a[92:95], a[252:255], v[172:175]\n"
    "ds_read_b128 a[224:227], v21 offset:41984\n"
    "buffer_load_dwordx4 v18, s[48:51], 0 offen offset:1024 lds\n"
    "v_mfma_f32_16x16x32_bf16 v[172:175], a[84:87], a[248:251], v[172:175]\n"
    "s_add_u32 m0, s57, 90112\n"
    "ds_read_b128 a[228:231], v22 offset:41984\n"
    "v_add_u32_dpp v19, v15, v2 row_newbcast:4 row_mask:0xf bank_mask:0xf\n"
    "v_mfma_f32_16x16x32_bf16 v[172:175], a[76:79], a[244:247], v[172:175]\n"
    "ds_read_b128 a[232:235], v23 offset:41984\n"
    "buffer_load_dwordx4 v19, s[48:51], 0 offen offset:0 lds\n"
    "v_mfma_f32_16x16x32_bf16 v[172:175], a[68:71], a[240:243], v[172:175]\n"
    "ds_read_b128 a[236:239], v24 offset:41984\n"
    "v_add_u32_dpp v20, v15, v3 row_newbcast:12 row_mask:0xf bank_mask:0xf\n"
    "v_mfma_f32_16x16x32_bf16 v[180:183], a[68:71], v[232:235], v[180:183]\n"
    "ds_read_b128 a[240:243], v21 offset:49152\n"
    "buffer_load_dwordx4 v20, s[48:51], 0 offen offset:1024 lds\n"
    "v_mfma_f32_16x16x32_bf16 v[180:183], a[76:79], v[236:239], v[180:183]\n"
    "s_add_u32 m0, s57, 98304\n"
    "ds_read_b128 a[244:247], v22 offset:49152\n"
    "v_add_u32_dpp v17, v15, v2 row_newbcast:5 row_mask:0xf bank_mask:0xf\n"
    "v_mfma_f32_16x16x32_bf16 v[180:183], a[84:87], v[240:243], v[180:183]\n"
    "ds_read_b128 a[248:251], v23 offset:49152\n"
    "buffer_load_dwordx4 v17, s[48:51], 0 offen offset:0 lds\n"
    "v_mfma_f32_16x16x32_bf16 v[180:183], a[92:95], v[244:247], v[180:183]\n"
    "ds_read_b128 a[252:255], v24 offset:49152\n"
    "v_add_u32_dpp v18, v15, v3 row_newbcast:13 row_mask:0xf bank_mask:0xf\n"
    "v_mfma_f32_16x16x32_bf16 v[176:179], a[88:91], v[244:247], v[176:179]\n"
    "buffer_load_dwordx4 v18, s[48:51], 0 offen offset:1024 lds\n"
    "v_cvt_pk_bf16_f32 v168, v168, v169\n"
    "v_mfma_f32_16x16x32_bf16 v[176:179], a[80:83], v[240:243], v[176:179]\n"
    "v_cvt_pk_bf16_f32 v169, v170, v171\n"
    "v_cvt_pk_bf16_f32 v170, v172, v173\n"
    "v_mfma_f32_16x16x32_bf16 v[176:179], a[72:75], v[236:239], v[176:179]\n"
    "v_cvt_pk_bf16_f32 v171, v174, v175\n"
    "v_mfma_f32_16x16x32_bf16 v[176:179], a[64:67], v[232:235], v[176:179]\n"
    "s_waitcnt lgkmcnt(3)\n"
    "buffer_store_dwordx4 v[168:171], v53, s[52:55], s36 offen offset:0 sc1\n"
    "v_mfma_f32_16x16x32_bf16 v[184:187], a[96:99], a[208:211], v[184:187]\n"
    "ds_read_b128 v[232:235], v21 offset:50176\n"
    "v_mfma_f32_16x16x32_bf16 v[184:187], a[104:107], a[212:215], v[184:187]\n"
    "ds_read_b128 v[236:239], v22 offset:50176\n"
    "v_mfma_f32_16x16x32_bf16 v[184:187], a[112:115], a[216:219], v[184:187]\n"
    "ds_read_b128 v[240:243], v23 offset:50176\n"
    "v_mfma_f32_16x16x32_bf16 v[184:187], a[120:123], a[220:223], v[184:187]\n"
    "ds_read_b128 v[244:247], v24 offset:50176\n"
    "v_cvt_pk_bf16_f32 v176, v176, v177\n"
    "v_mfma_f32_16x16x32_bf16 v[188:191], a[124:127], a[220:223], v[188:191]\n"
    "v_cvt_pk_bf16_f32 v177, v178, v179\n"
    "v_cvt_pk_bf16_f32 v178, v180, v181\n"
    "v_mfma_f32_16x16x32_bf16 v[188:191], a[116:119], a[216:219], v[188:191]\n"
    "v_cvt_pk_bf16_f32 v179, v182, v183\n"
    "v_pk_add_f32 v[200:201], v[200:201], v[84:85]\n"
    "v_mfma_f32_16x16x32_bf16 v[188:191], a[108:111], a[212:215], v[188:191]\n"
    "v_pk_add_f32 v[202:203], v[202:203], v[86:87]\n"
    "v_pk_add_f32 v[204:205], v[204:205], v[88:89]\n"
    "v_mfma_f32_16x16x32_bf16 v[188:191], a[100:103], a[208:211], v[188:191]\n"
    "v_pk_add_f32 v[206:207], v[206:207], v[90:91]\n"
    "v_pk_add_f32 v[208:209], v[208:209], v[84:85]\n"
    "v_mfma_f32_16x16x32_bf16 v[196:199], a[100:103], a[224:227], v[196:199]\n"
    "v_pk_add_f32 v[210:211], v[210:211], v[86:87]\n"
    "v_pk_add_f32 v[212:213], v[212:213], v[88:89]\n"
    "v_mfma_f32_16x16x32_bf16 v[196:199], a[108:111], a[228:231], v[196:199]\n"
    "v_pk_add_f32 v[214:215], v[214:215], v[90:91]\n"
    "v_pk_add_f32 v[216:217], v[216:217], v[84:85]\n"
    "v_mfma_f32_16x16x32_bf16 v[196:199], a[116:119], a[232:235], v[196:199]\n"
    "v_pk_add_f32 v[218:219], v[218:219], v[86:87]\n"
    "v_pk_add_f32 v[220:221], v[220:221], v[88:89]\n"
    "v_mfma_f32_16x16x32_bf16 v[196:199], a[124:127], a[236:239], v[196:199]\n"
    "v_pk_add_f32 v[222:223], v[222:223], v[90:91]\n"
    "v_pk_add_f32 v[224:225], v[224:225], v[84:85]\n"
    "v_mfma_f32_16x16x32_bf16 v[192:195], a[120:123], a[236:239], v[192:195]\n"
    "v_pk_add_f32 v[226:227], v[226:227], v[86:87]\n"
    "v_pk_add_f32 v[228:229], v[228:229], v[88:89]\n"
    "v_mfma_f32_16x16x32_bf16 v[192:195], a[112:115], a[232:235], v[192:195]\n"
    "v_pk_add_f32 v[230:231], v[230:231], v[90:91]\n"
    "v_mfma_f32_16x16x32_bf16 v[192:195], a[104:107], a[228:231], v[192:195]\n"
    "v_mfma_f32_16x16x32_bf16 v[192:195], a[96:99], a[224:227], v[192:195]\n"
    "s_waitcnt vmcnt(12) lgkmcnt(0)\n"
    "s_barrier\n"
    "v_mfma_f32_16x16x32_bf16 v[184:187], a[64:67], a[240:243], v[184:187]\n"
    "buffer_store_dwordx4 v[176:179], v53, s[52:55], s36 offen offset:1024 sc1\n"
    "v_mfma_f32_16x16x32_bf16 v[184:187], a[72:75], a[244:247], v[184:187]\n"
    "ds_read_b128 a[208:211], v21 offset:57344\n"
    "v_mfma_f32_16x16x32_bf16 v[184:187], a[80:83], a[248:251], v[184:187]\n"
    "ds_read_b128 a[212:215], v22 offset:57344\n"
    "v_mfma_f32_16x16x32_bf16 v[184:187], a[88:91], a[252:255], v[184:187]\n"
    "ds_read_b128 a[216:219], v23 offset:57344\n"
    "v_mfma_f32_16x16x32_bf16 v[188:191], a[92:95], a[252:255], v[188:191]\n"
    "ds_read_b128 a[220:223], v24 offset:57344\n"
    "v_mfma_f32_16x16x32_bf16 v[188:191], a[84:87], a[248:251], v[188:191]\n"
    "ds_read_b128 a[224:227], v21 offset:58368\n"
    "v_mfma_f32_16x16x32_bf16 v[188:191], a[76:79], a[244:247], v[188:191]\n"
    "ds_read_b128 a[228:231], v22 offset:58368\n"
    "v_mfma_f32_16x16x32_bf16 v[188:191], a[68:71], a[240:243], v[188:191]\n"
    "ds_read_b128 a[232:235], v23 offset:58368\n"
    "v_mfma_f32_16x16x32_bf16 v[196:199], a[68:71], v[232:235], v[196:199]\n"
    "ds_read_b128 a[236:239], v24 offset:58368\n"
    "v_mfma_f32_16x16x32_bf16 v[196:199], a[76:79], v[236:239], v[196:199]\n"
    "ds_read_b128 a[240:243], v25 offset:8192\n"
    "v_mfma_f32_16x16x32_bf16 v[196:199], a[84:87], v[240:243], v[196:199]\n"
    "ds_read_b128 a[244:247], v26 offset:8192\n"
    "v_mfma_f32_16x16x32_bf16 v[196:199], a[92:95], v[244:247], v[196:199]\n"
    "ds_read_b128 a[248:251], v27 offset:8192\n"
    "v_mfma_f32_16x16x32_bf16 v[192:195], a[88:91], v[244:247], v[192:195]\n"
    "ds_read_b128 a[252:255], v28 offset:8192\n"
    "v_mfma_f32_16x16x32_bf16 v[192:195], a[80:83], v[240:243], v[192:195]\n"
    "v_mfma_f32_16x16x32_bf16 v[192:195], a[72:75], v[236:239], v[192:195]\n"
    "v_mfma_f32_16x16x32_bf16 v[192:195], a[64:67], v[232:235], v[192:195]\n"
    "s_waitcnt lgkmcnt(4)\n"
    "ds_read_b128 v[232:235], v25 offset:9216\n"
    "v_mfma_f32_16x16x32_bf16 v[184:187], a[160:163], a[208:211], v[184:187]\n"
    "ds_read_b128 v[236:239], v26 offset:9216\n"
    "v_mfma_f32_16x16x32_bf16 v[184:187], a[168:171], a[212:215], v[184:187]\n"
    "ds_read_b128 v[240:243], v27 offset:9216\n"
    "v_mfma_f32_16x16x32_bf16 v[184:187], a[176:179], a[216:219], v[184:187]\n"
    "ds_read_b128 v[244:247], v28 offset:9216\n"
    "v_mfma_f32_16x16x32_bf16 v[184:187], a[184:187], a[220:223], v[184:187]\n"
    "v_mfma_f32_16x16x32_bf16 v[188:191], a[188:191], a[220:223], v[188:191]\n"
    "v_mfma_f32_16x16x32_bf16 v[188:191], a[180:183], a[216:219], v[188:191]\n"
    "v_mfma_f32_16x16x32_bf16 v[188:191], a[172:175], a[212:215], v[188:191]\n"
    "v_mfma_f32_16x16x32_bf16 v[188:191], a[164:167], a[208:211], v[188:191]\n"
    "v_mfma_f32_16x16x32_bf16 v[196:199], a[164:167], a[224:227], v[196:199]\n"
    "v_mfma_f32_16x16x32_bf16 v[196:199], a[172:175], a[228:231], v[196:199]\n"
    "v_mfma_f32_16x16x32_bf16 v[196:199], a[180:183], a[232:235], v[196:199]\n"
    "v_mfma_f32_16x16x32_bf16 v[196:199], a[188:191], a[236:239], v[196:199]\n"
    "v_mfma_f32_16x16x32_bf16 v[192:195], a[184:187], a[236:239], v[192:195]\n"
    "v_mfma_f32_16x16x32_bf16 v[192:195], a[176:179], a[232:235], v[192:195]\n"
    "s_waitcnt vmcnt(9)\n"
    "s_xor_b32 s62, s59, 512\n"
    "v_mfma_f32_16x16x32_bf16 v[192:195], a[168:171], a[228:231], v[192:195]\n"
    "buffer_store_dword v57, off, s[52:55], s62 offset:208 sc1\n"
    "v_mfma_f32_16x16x32_bf16 v[192:195], a[160:163], a[224:227], v[192:195]\n"
    "s_waitcnt lgkmcnt(0)\n"
    "s_barrier\n"
    "v_mfma_f32_16x16x32_bf16 v[184:187], a[128:131], a[240:243], v[184:187]\n"
    "ds_read_b128 a[208:211], v29 offset:0\n"
    "v_mfma_f32_16x16x32_bf16 v[184:187], a[136:139], a[244:247], v[184:187]\n"
    "ds_read_b128 a[212:215], v30 offset:0\n"
    "v_mfma_f32_16x16x32_bf16 v[184:187], a[144:147], a[248:251], v[184:187]\n"
    "ds_read_b128 a[216:219], v31 offset:0\n"
    "v_mfma_f32_16x16x32_bf16 v[184:187], a[152:155], a[252:255], v[184:187]\n"
    "ds_read_b128 a[220:223], v32 offset:0\n"
    "v_mfma_f32_16x16x32_bf16 v[188:191], a[156:159], a[252:255], v[188:191]\n"
    "ds_read_b128 a[224:227], v29 offset:1024\n"
    "v_mfma_f32_16x16x32_bf16 v[188:191], a[148:151], a[248:251], v[188:191]\n"
    "ds_read_b128 a[228:231], v30 offset:1024\n"
    "v_mfma_f32_16x16x32_bf16 v[188:191], a[140:143], a[244:247], v[188:191]\n"
    "ds_read_b128 a[232:235], v31 offset:1024\n"
    "v_mfma_f32_16x16x32_bf16 v[188:191], a[132:135], a[240:243], v[188:191]\n"
    "ds_read_b128 a[236:239], v32 offset:1024\n"
    "v_mfma_f32_16x16x32_bf16 v[196:199], a[132:135], v[232:235], v[196:199]\n"
    "ds_read_b128 a[240:243], v29 offset:8192\n"
    "v_mfma_f32_16x16x32_bf16 v[196:199], a[140:143], v[236:239], v[196:199]\n"
    "ds_read_b128 a[244:247], v30 offset:8192\n"
    "v_mfma_f32_16x16x32_bf16 v[196:199], a[148:151], v[240:243], v[196:199]\n"
    "ds_read_b128 a[248:251], v31 offset:8192\n"
    "buffer_load_dword v59, off, s[52:55], s59 offset:192 sc1\n"
    "v_mfma_f32_16x16x32_bf16 v[196:199], a[156:159], v[244:247], v[196:199]\n"
    "ds_read_b128 a[252:255], v32 offset:8192\n"
    "v_cvt_pk_bf16_f32 v184, v184, v185\n"
    "v_mfma_f32_16x16x32_bf16 v[192:195], a[152:155], v[244:247], v[192:195]\n"
    "v_cvt_pk_bf16_f32 v185, v186, v187\n"
    "v_cvt_pk_bf16_f32 v186, v188, v189\n"
    "v_mfma_f32_16x16x32_bf16 v[192:195], a[144:147], v[240:243], v[192:195]\n"
    "v_cvt_pk_bf16_f32 v187, v190, v191\n"
    "v_mfma_f32_16x16x32_bf16 v[192:195], a[136:139], v[236:239], v[192:195]\n"
    "buffer_store_dwordx4 v[184:187], v53, s[52:55], s36 offen offset:2048 sc1\n"
    "v_mfma_f32_16x16x32_bf16 v[192:195], a[128:131], v[232:235], v[192:195]\n"
    "s_waitcnt lgkmcnt(3)\n"
    "ds_read_b128 v[232:235], v29 offset:9216\n"
    "v_mfma_f32_16x16x32_bf16 v[200:203], a[0:3], a[208:211], v[200:203]\n"
    "ds_read_b128 v[236:239], v30 offset:9216\n"
    "v_mfma_f32_16x16x32_bf16 v[200:203], a[8:11], a[212:215], v[200:203]\n"
    "ds_read_b128 v[240:243], v31 offset:9216\n"
    "v_mfma_f32_16x16x32_bf16 v[200:203], a[16:19], a[216:219], v[200:203]\n"
    "ds_read_b128 v[244:247], v32 offset:9216\n"
    "v_mfma_f32_16x16x32_bf16 v[200:203], a[24:27], a[220:223], v[200:203]\n"
    "v_cvt_pk_bf16_f32 v192, v192, v193\n"
    "v_cvt_pk_bf16_f32 v193, v194, v195\n"
    "v_mfma_f32_16x16x32_bf16 v[204:207], a[28:31], a[220:223], v[204:207]\n"
    "v_cvt_pk_bf16_f32 v194, v196, v197\n"
    "v_cvt_pk_bf16_f32 v195, v198, v199\n"
    "v_mfma_f32_16x16x32_bf16 v[204:207], a[20:23], a[216:219], v[204:207]\n"
    "v_mfma_f32_16x16x32_bf16 v[204:207], a[12:15], a[212:215], v[204:207]\n"
    "v_mfma_f32_16x16x32_bf16 v[204:207], a[4:7], a[208:211], v[204:207]\n"
    "v_mfma_f32_16x16x32_bf16 v[212:215], a[4:7], a[224:227], v[212:215]\n"
    "v_mfma_f32_16x16x32_bf16 v[212:215], a[12:15], a[228:231], v[212:215]\n"
    "v_mfma_f32_16x16x32_bf16 v[212:215], a[20:23], a[232:235], v[212:215]\n"
    "v_mfma_f32_16x16x32_bf16 v[212:215], a[28:31], a[236:239], v[212:215]\n"
    "v_mfma_f32_16x16x32_bf16 v[208:211], a[24:27], a[236:239], v[208:211]\n"
    "v_mfma_f32_16x16x32_bf16 v[208:211], a[16:19], a[232:235], v[208:211]\n"
    "v_mfma_f32_16x16x32_bf16 v[208:211], a[8:11], a[228:231], v[208:211]\n"
    "buffer_store_dwordx4 v[192:195], v53, s[52:55], s36 offen offset:3072 sc1\n"
    "v_mfma_f32_16x16x32_bf16 v[208:211], a[0:3], a[224:227], v[208:211]\n"
    "s_waitcnt vmcnt(12) lgkmcnt(0)\n"
    "s_barrier\n"
    "v_mfma_f32_16x16x32_bf16 v[216:219], a[0:3], a[240:243], v[216:219]\n"
    "ds_read_b128 a[208:211], v29 offset:16384\n"
    "v_mfma_f32_16x16x32_bf16 v[216:219], a[8:11], a[244:247], v[216:219]\n"
    "ds_read_b128 a[212:215], v30 offset:16384\n"
    "v_mfma_f32_16x16x32_bf16 v[216:219], a[16:19], a[248:251], v[216:219]\n"
    "ds_read_b128 a[216:219], v31 offset:16384\n"
    "v_mfma_f32_16x16x32_bf16 v[216:219], a[24:27], a[252:255], v[216:219]\n"
    "ds_read_b128 a[220:223], v32 offset:16384\n"
    "v_mfma_f32_16x16x32_bf16 v[220:223], a[28:31], a[252:255], v[220:223]\n"
    "ds_read_b128 a[224:227], v29 offset:17408\n"
    "v_mfma_f32_16x16x32_bf16 v[220:223], a[20:23], a[248:251], v[220:223]\n"
    "ds_read_b128 a[228:231], v30 offset:17408\n"
    "v_mfma_f32_16x16x32_bf16 v[220:223], a[12:15], a[244:247], v[220:223]\n"
    "ds_read_b128 a[232:235], v31 offset:17408\n"
    "v_mfma_f32_16x16x32_bf16 v[220:223], a[4:7], a[240:243], v[220:223]\n"
    "ds_read_b128 a[236:239], v32 offset:17408\n"
    "v_mfma_f32_16x16x32_bf16 v[228:231], a[4:7], v[232:235], v[228:231]\n"
    "s_add_u32 m0, s57, 106496\n"
    "ds_read_b128 a[240:243], v25 offset:16384\n"
    "v_add_u32_dpp v19, v16, v2 row_newbcast:1 row_mask:0xf bank_mask:0xf\n"
    "v_mfma_f32_16x16x32_bf16 v[228:231], a[12:15], v[236:239], v[228:231]\n"
    "ds_read_b128 a[244:247], v26 offset:16384\n"
    "v_mfma_f32_16x16x32_bf16 v[228:231], a[20:23], v[240:243], v[228:231]\n"
    "ds_read_b128 a[248:251], v27 offset:16384\n"
    "v_mfma_f32_16x16x32_bf16 v[228:231], a[28:31], v[244:247], v[228:231]\n"
    "ds_read_b128 a[252:255], v28 offset:16384\n"
    "v_mfma_f32_16x16x32_bf16 v[224:227], a[24:27], v[244:247], v[224:227]\n"
    "buffer_load_dwordx4 v19, s[48:51], 0 offen offset:0 lds\n"
    "v_add_u32_dpp v20, v16, v3 row_newbcast:9 row_mask:0xf bank_mask:0xf\n"
    "v_mfma_f32_16x16x32_bf16 v[224:227], a[16:19], v[240:243], v[224:227]\n"
    "buffer_load_dwordx4 v20, s[48:51], 0 offen offset:1024 lds\n"
    "s_add_u32 m0, s57, 114688\n"
    "v_mfma_f32_16x16x32_bf16 v[224:227], a[8:11], v[236:239], v[224:227]\n"
    "v_add_u32_dpp v17, v16, v2 row_newbcast:3 row_mask:0xf bank_mask:0xf\n"
    "v_mfma_f32_16x16x32_bf16 v[224:227], a[0:3], v[232:235], v[224:227]\n"
    "s_waitcnt lgkmcnt(3)\n"
    "ds_read_b128 v[232:235], v25 offset:17408\n"
    "buffer_load_dwordx4 v17, s[48:51], 0 offen offset:0 lds\n"
    "v_mfma_f32_16x16x32_bf16 v[200:203], a[64:67], a[208:211], v[200:203]\n"
    "ds_read_b128 v[236:239], v26 offset:17408\n"
    "v_add_u32_dpp v18, v16, v3 row_newbcast:11 row_mask:0xf bank_mask:0xf\n"
    "v_mfma_f32_16x16x32_bf16 v[200:203], a[72:75], a[212:215], v[200:203]\n"
    "ds_read_b128 v[240:243], v27 offset:17408\n"
    "buffer_load_dwordx4 v18, s[48:51], 0 offen offset:1024 lds\n"
    "v_mfma_f32_16x16x32_bf16 v[200:203], a[80:83], a[216:219], v[200:203]\n"
    "s_add_u32 m0, s57, 122880\n"
    "ds_read_b128 v[244:247], v28 offset:17408\n"
    "v_add_u32_dpp v19, v15, v2 row_newbcast:3 row_mask:0xf bank_mask:0xf\n"
    "v_mfma_f32_16x16x32_bf16 v[200:203], a[88:91], a[220:223], v[200:203]\n"
    "v_mfma_f32_16x16x32_bf16 v[204:207], a[92:95], a[220:223], v[204:207]\n"
    "v_mfma_f32_16x16x32_bf16 v[204:207], a[84:87], a[216:219], v[204:207]\n"
    "buffer_load_dwordx4 v19, s[48:51], 0 offen offset:0 lds\n"
    "v_add_u32_dpp v20, v15, v3 row_newbcast:11 row_mask:0xf bank_mask:0xf\n"
    "v_mfma_f32_16x16x32_bf16 v[204:207], a[76:79], a[212:215], v[204:207]\n"
    "v_mfma_f32_16x16x32_bf16 v[204:207], a[68:71], a[208:211], v[204:207]\n"
    "v_mfma_f32_16x16x32_bf16 v[212:215], a[68:71], a[224:227], v[212:215]\n"
    "v_mfma_f32_16x16x32_bf16 v[212:215], a[76:79], a[228:231], v[212:215]\n"
    "v_mfma_f32_16x16x32_bf16 v[212:215], a[84:87], a[232:235], v[212:215]\n"
    "v_mfma_f32_16x16x32_bf16 v[212:215], a[92:95], a[236:239], v[212:215]\n"
    "v_mfma_f32_16x16x32_bf16 v[208:211], a[88:91], a[236:239], v[208:211]\n"
    "v_mfma_f32_16x16x32_bf16 v[208:211], a[80:83], a[232:235], v[208:211]\n"
    "v_mfma_f32_16x16x32_bf16 v[208:211], a[72:75], a[228:231], v[208:211]\n"
    "v_mfma_f32_16x16x32_bf16 v[208:211], a[64:67], a[224:227], v[208:211]\n"
    "s_waitcnt vmcnt(13) lgkmcnt(0)\n"
    "s_barrier\n"
    "v_mfma_f32_16x16x32_bf16 v[200:203], a[32:35], a[240:243], v[200:203]\n"
    "ds_read_b128 a[208:211], v25 offset:0\n"
    "buffer_load_dwordx4 v20, s[48:51], 0 offen offset:1024 lds\n"
    "v_mfma_f32_16x16x32_bf16 v[200:203], a[40:43], a[244:247], v[200:203]\n"
    "ds_read_b128 a[212:215], v26 offset:0\n"
    "v_mfma_f32_16x16x32_bf16 v[200:203], a[48:51], a[248:251], v[200:203]\n"
    "ds_read_b128 a[216:219], v27 offset:0\n"
    "v_mfma_f32_16x16x32_bf16 v[200:203], a[56:59], a[252:255], v[200:203]\n"
    "ds_read_b128 a[220:223], v28 offset:0\n"
    "v_mfma_f32_16x16x32_bf16 v[204:207], a[60:63], a[252:255], v[204:207]\n"
    "ds_read_b128 a[224:227], v25 offset:1024\n"
    "v_mfma_f32_16x16x32_bf16 v[204:207], a[52:55], a[248:251], v[204:207]\n"
    "ds_read_b128 a[228:231], v26 offset:1024\n"
    "v_mfma_f32_16x16x32_bf16 v[204:207], a[44:47], a[244:247], v[204:207]\n"
    "ds_read_b128 a[232:235], v27 offset:1024\n"
    "v_mfma_f32_16x16x32_bf16 v[204:207], a[36:39], a[240:243], v[204:207]\n"
    "s_add_u32 m0, s57, 155648\n"
    "ds_read_b128 a[236:239], v28 offset:1024\n"
    "v_add_u32_dpp v17, v16, v2 row_newbcast:4 row_mask:0xf bank_mask:0xf\n"
    "v_mfma_f32_16x16x32_bf16 v[212:215], a[36:39], v[232:235], v[212:215]\n"
    "ds_read_b128 a[240:243], v25 offset:24576\n"
    "v_mfma_f32_16x16x32_bf16 v[212:215], a[44:47], v[236:239], v[212:215]\n"
    "ds_read_b128 a[244:247], v26 offset:24576\n"
    "v_mfma_f32_16x16x32_bf16 v[212:215], a[52:55], v[240:243], v[212:215]\n"
    "ds_read_b128 a[248:251], v27 offset:24576\n"
    "buffer_load_dwordx4 v17, s[48:51], 0 offen offset:0 lds\n"
    "v_mfma_f32_16x16x32_bf16 v[212:215], a[60:63], v[244:247], v[212:215]\n"
    "ds_read_b128 a[252:255], v28 offset:24576\n"
    "v_add_u32_dpp v18, v16, v3 row_newbcast:12 row_mask:0xf bank_mask:0xf\n"
    "v_mfma_f32_16x16x32_bf16 v[208:211], a[56:59], v[244:247], v[208:211]\n"
    "buffer_load_dwordx4 v18, s[48:51], 0 offen offset:1024 lds\n"
    "s_add_u32 m0, s57, 0\n"
    "v_mfma_f32_16x16x32_bf16 v[208:211], a[48:51], v[240:243], v[208:211]\n"
    "v_add_u32_dpp v19, v16, v2 row_newbcast:5 row_mask:0xf bank_mask:0xf\n"
    "v_mfma_f32_16x16x32_bf16 v[208:211], a[40:43], v[236:239], v[208:211]\n"
    "v_mfma_f32_16x16x32_bf16 v[208:211], a[32:35], v[232:235], v[208:211]\n"
    "s_waitcnt lgkmcnt(3)\n"
    "ds_read_b128 v[232:235], v25 offset:25600\n"
    "buffer_load_dwordx4 v19, s[48:51], 0 offen offset:0 lds\n"
    "v_mfma_f32_16x16x32_bf16 v[216:219], a[64:67], a[208:211], v[216:219]\n"
    "ds_read_b128 v[236:239], v26 offset:25600\n"
    "v_add_u32_dpp v20, v16, v3 row_newbcast:13 row_mask:0xf bank_mask:0xf\n"
    "v_mfma_f32_16x16x32_bf16 v[216:219], a[72:75], a[212:215], v[216:219]\n"
    "ds_read_b128 v[240:243], v27 offset:25600\n"
    "v_mfma_f32_16x16x32_bf16 v[216:219], a[80:83], a[216:219], v[216:219]\n"
    "ds_read_b128 v[244:247], v28 offset:25600\n"
    "buffer_load_dword v60, off, s[52:55], s59 offset:208 sc1\n"
    "v_mfma_f32_16x16x32_bf16 v[216:219], a[88:91], a[220:223], v[216:219]\n"
    "v_mfma_f32_16x16x32_bf16 v[220:223], a[92:95], a[220:223], v[220:223]\n"
    "v_mfma_f32_16x16x32_bf16 v[220:223], a[84:87], a[216:219], v[220:223]\n"
    "v_mfma_f32_16x16x32_bf16 v[220:223], a[76:79], a[212:215], v[220:223]\n"
    "buffer_load_dwordx4 v20, s[48:51], 0 offen offset:1024 lds\n"
    "v_mfma_f32_16x16x32_bf16 v[220:223], a[68:71], a[208:211], v[220:223]\n"
    "v_mfma_f32_16x16x32_bf16 v[228:231], a[68:71], a[224:227], v[228:231]\n"
    "v_mfma_f32_16x16x32_bf16 v[228:231], a[76:79], a[228:231], v[228:231]\n"
    "v_mfma_f32_16x16x32_bf16 v[228:231], a[84:87], a[232:235], v[228:231]\n"
    "s_waitcnt vmcnt(13)\n"
    "v_cmp_ne_u32 vcc, 1, v59\n"
    "v_mfma_f32_16x16x32_bf16 v[228:231], a[92:95], a[236:239], v[228:231]\n"
    "s_cbranch_vccnz .Lwf0_%=\n"
    ".Lokf0_%=:\n"
    "s_add_u32 s62, s98, 0\n"
    "v_mfma_f32_16x16x32_bf16 v[224:227], a[88:91], a[236:239], v[224:227]\n"
    "buffer_load_dwordx4 v[108:111], v53, s[52:55], s62 offen offset:0 sc1\n"
    "v_mfma_f32_16x16x32_bf16 v[224:227], a[80:83], a[232:235], v[224:227]\n"
    "buffer_load_dwordx4 v[116:119], v53, s[52:55], s62 offen offset:1024 sc1\n"
    "v_mfma_f32_16x16x32_bf16 v[224:227], a[72:75], a[228:231], v[224:227]\n"
    "buffer_load_dwordx4 v[124:127], v53, s[52:55], s62 offen offset:2048 sc1\n"
    "v_mfma_f32_16x16x32_bf16 v[224:227], a[64:67], a[224:227], v[224:227]\n"
    "s_waitcnt vmcnt(12)\n"
    "s_barrier\n"
    "v_mfma_f32_16x16x32_bf16 v[200:203], a[128:131], a[240:243], v[200:203]\n"
    "s_waitcnt lgkmcnt(0)\n"
    "ds_read_b128 a[208:211], v25 offset:32768\n"
    "buffer_load_dwordx4 v[132:135], v53, s[52:55], s62 offen offset:3072 sc1\n"
    "v_mfma_f32_16x16x32_bf16 v[200:203], a[136:139], a[244:247], v[200:203]\n"
    "ds_read_b128 a[212:215], v26 offset:32768\n"
    "s_xor_b32 s62, s59, 768\n"
    "v_mfma_f32_16x16x32_bf16 v[200:203], a[144:147], a[248:251], v[200:203]\n"
    "ds_read_b128 a[216:219], v27 offset:32768\n"
    "buffer_store_dword v57, off, s[52:55], s62 offset:224 sc1\n"
    "v_mfma_f32_16x16x32_bf16 v[200:203], a[152:155], a[252:255], v[200:203]\n"
    "ds_read_b128 a[220:223], v28 offset:32768\n"
    "v_mfma_f32_16x16x32_bf16 v[204:207], a[156:159], a[252:255], v[204:207]\n"
    "ds_read_b128 a[224:227], v25 offset:33792\n"
    "v_mfma_f32_16x16x32_bf16 v[204:207], a[148:151], a[248:251], v[204:207]\n"
    "ds_read_b128 a[228:231], v26 offset:33792\n"
    "v_mfma_f32_16x16x32_bf16 v[204:207], a[140:143], a[244:247], v[204:207]\n"
    "ds_read_b128 a[232:235], v27 offset:33792\n"
    "v_mfma_f32_16x16x32_bf16 v[204:207], a[132:135], a[240:243], v[204:207]\n"
    "ds_read_b128 a[236:239], v28 offset:33792\n"
    "v_mfma_f32_16x16x32_bf16 v[212:215], a[132:135], v[232:235], v[212:215]\n"
    "ds_read_b128 a[240:243], v25 offset:40960\n"
    "v_mfma_f32_16x16x32_bf16 v[212:215], a[140:143], v[236:239], v[212:215]\n"
    "ds_read_b128 a[244:247], v26 offset:40960\n"
    "v_mfma_f32_16x16x32_bf16 v[212:215], a[148:151], v[240:243], v[212:215]\n"
    "ds_read_b128 a[248:251], v27 offset:40960\n"
    "v_mfma_f32_16x16x32_bf16 v[212:215], a[156:159], v[244:247], v[212:215]\n"
    "ds_read_b128 a[252:255], v28 offset:40960\n"
    "v_mfma_f32_16x16x32_bf16 v[208:211], a[152:155], v[244:247], v[208:211]\n"
    "v_mfma_f32_16x16x32_bf16 v[208:211], a[144:147], v[240:243], v[208:211]\n"
    "v_mfma_f32_16x16x32_bf16 v[208:211], a[136:139], v[236:239], v[208:211]\n"
    "v_mfma_f32_16x16x32_bf16 v[208:211], a[128:131], v[232:235], v[208:211]\n"
    "s_waitcnt lgkmcnt(3)\n"
    "ds_read_b128 v[232:235], v25 offset:41984\n"
    "v_mfma_f32_16x16x32_bf16 v[200:203], a[160:163], a[208:211], v[200:203]\n"
    "ds_read_b128 v[236:239], v26 offset:41984\n"
    "v_mfma_f32_16x16x32_bf16 v[200:203], a[168:171], a[212:215], v[200:203]\n"
    "ds_read_b128 v[240:243], v27 offset:41984\n"
    "v_mfma_f32_16x16x32_bf16 v[200:203], a[176:179], a[216:219], v[200:203]\n"
    "ds_read_b128 v[244:247], v28 offset:41984\n"
    "v_mfma_f32_16x16x32_bf16 v[200:203], a[184:187], a[220:223], v[200:203]\n"
    "v_mfma_f32_16x16x32_bf16 v[204:207], a[188:191], a[220:223], v[204:207]\n"
    "v_mfma_f32_16x16x32_bf16 v[204:207], a[180:183], a[216:219], v[204:207]\n"
    "v_mfma_f32_16x16x32_bf16 v[204:207], a[172:175], a[212:215], v[204:207]\n"
    "v_mfma_f32_16x16x32_bf16 v[204:207], a[164:167], a[208:211], v[204:207]\n"
    "v_mfma_f32_16x16x32_bf16 v[212:215], a[164:167], a[224:227], v[212:215]\n"
    "v_mfma_f32_16x16x32_bf16 v[212:215], a[172:175], a[228:231], v[212:215]\n"
    "v_mfma_f32_16x16x32_bf16 v[212:215], a[180:183], a[232:235], v[212:215]\n"
    "v_mfma_f32_16x16x32_bf16 v[212:215], a[188:191], a[236:239], v[212:215]\n"
    "v_mfma_f32_16x16x32_bf16 v[208:211], a[184:187], a[236:239], v[208:211]\n"
    "v_mfma_f32_16x16x32_bf16 v[208:211], a[176:179], a[232:235], v[208:211]\n"
    "buffer_load_dword v61, off, s[52:55], s59 offset:224 sc1\n"
    "v_mfma_f32_16x16x32_bf16 v[208:211], a[168:171], a[228:231], v[208:211]\n"
    "v_mfma_f32_16x16x32_bf16 v[208:211], a[160:163], a[224:227], v[208:211]\n"
    "s_waitcnt vmcnt(11)\n"
    "s_barrier\n"
    "v_mfma_f32_16x16x32_bf16 v[216:219], a[32:35], a[240:243], v[216:219]\n"
    "s_waitcnt lgkmcnt(0)\n"
    "ds_read_b128 a[208:211], v25 offset:49152\n"
    "v_mfma_f32_16x16x32_bf16 v[216:219], a[40:43], a[244:247], v[216:219]\n"
    "ds_read_b128 a[212:215], v26 offset:49152\n"
    "v_mfma_f32_16x16x32_bf16 v[216:219], a[48:51], a[248:251], v[216:219]\n"
    "ds_read_b128 a[216:219], v27 offset:49152\n"
    "v_mfma_f32_16x16x32_bf16 v[216:219], a[56:59], a[252:255], v[216:219]\n"
    "ds_read_b128 a[220:223], v28 offset:49152\n"
    "v_mfma_f32_16x16x32_bf16 v[220:223], a[60:63], a[252:255], v[220:223]\n"
    "ds_read_b128 a[224:227], v25 offset:50176\n"
    "v_mfma_f32_16x16x32_bf16 v[220:223], a[52:55], a[248:251], v[220:223]\n"
    "ds_read_b128 a[228:231], v26 offset:50176\n"
    "v_mfma_f32_16x16x32_bf16 v[220:223], a[44:47], a[244:247], v[220:223]\n"
    "ds_read_b128 a[232:235], v27 offset:50176\n"
    "v_mfma_f32_16x16x32_bf16 v[220:223], a[36:39], a[240:243], v[220:223]\n"
    "ds_read_b128 a[236:239], v28 offset:50176\n"
    "v_mfma_f32_16x16x32_bf16 v[228:231], a[36:39], v[232:235], v[228:231]\n"
    "s_waitcnt vmcnt(7)\n"
    "ds_read_b128 a[240:243], v25 offset:57344\n"
    "v_cmp_ne_u32 vcc, 1, v60\n"
    "v_mfma_f32_16x16x32_bf16 v[228:231], a[44:47], v[236:239], v[228:231]\n"
    "s_cbranch_vccnz .Lwf1_%=\n"
    ".Lokf1_%=:\n"
    "ds_read_b128 a[244:247], v26 offset:57344\n"
    "s_add_u32 s62, s98, 16384\n"
    "v_mfma_f32_16x16x32_bf16 v[228:231], a[52:55], v[240:243], v[228:231]\n"
    "ds_read_b128 a[248:251], v27 offset:57344\n"
    "buffer_load_dwordx4 v[140:143], v53, s[52:55], s62 offen offset:0 sc1\n"
    "v_mfma_f32_16x16x32_bf16 v[228:231], a[60:63], v[244:247], v[228:231]\n"
    "ds_read_b128 a[252:255], v28 offset:57344\n"
    "buffer_load_dwordx4 v[148:151], v53, s[52:55], s62 offen offset:1024 sc1\n"
    "v_mfma_f32_16x16x32_bf16 v[224:227], a[56:59], v[244:247], v[224:227]\n"
    "buffer_load_dwordx4 v[156:159], v53, s[52:55], s62 offen offset:2048 sc1\n"
    "v_mfma_f32_16x16x32_bf16 v[224:227], a[48:51], v[240:243], v[224:227]\n"
    "buffer_load_dwordx4 v[164:167], v53, s[52:55], s62 offen offset:3072 sc1\n"
    "v_mfma_f32_16x16x32_bf16 v[224:227], a[40:43], v[236:239], v[224:227]\n"
    "v_mfma_f32_16x16x32_bf16 v[224:227], a[32:35], v[232:235], v[224:227]\n"
    "s_waitcnt lgkmcnt(3)\n"
    "ds_read_b128 v[232:235], v25 offset:58368\n"
    "v_mfma_f32_16x16x32_bf16 v[216:219], a[96:99], a[208:211], v[216:219]\n"
    "ds_read_b128 v[236:239], v26 offset:58368\n"
    "v_mfma_f32_16x16x32_bf16 v[216:219], a[104:107], a[212:215], v[216:219]\n"
    "ds_read_b128 v[240:243], v27 offset:58368\n"
    "v_mfma_f32_16x16x32_bf16 v[216:219], a[112:115], a[216:219], v[216:219]\n"
    "ds_read_b128 v[244:247], v28 offset:58368\n"
    "v_mfma_f32_16x16x32_bf16 v[216:219], a[120:123], a[220:223], v[216:219]\n"
    "v_mfma_f32_16x16x32_bf16 v[220:223], a[124:127], a[220:223], v[220:223]\n"
    "v_mfma_f32_16x16x32_bf16 v[220:223], a[116:119], a[216:219], v[220:223]\n"
    "v_mfma_f32_16x16x32_bf16 v[220:223], a[108:111], a[212:215], v[220:223]\n"
    "v_mfma_f32_16x16x32_bf16 v[220:223], a[100:103], a[208:211], v[220:223]\n"
    "v_mfma_f32_16x16x32_bf16 v[228:231], a[100:103], a[224:227], v[228:231]\n"
    "v_mfma_f32_16x16x32_bf16 v[228:231], a[108:111], a[228:231], v[228:231]\n"
    "v_mfma_f32_16x16x32_bf16 v[228:231], a[116:119], a[232:235], v[228:231]\n"
    "s_waitcnt vmcnt(9)\n"
    "v_mfma_f32_16x16x32_bf16 v[200:203], v[94:97], v[108:111], v[200:203]\n"
    "v_mfma_f32_16x16x32_bf16 v[204:207], v[92:95], v[108:111], v[204:207]\n"
    "s_waitcnt vmcnt(8)\n"
    "v_mfma_f32_16x16x32_bf16 v[208:211], v[94:97], v[116:119], v[208:211]\n"
    "v_mfma_f32_16x16x32_bf16 v[212:215], v[92:95], v[116:119], v[212:215]\n"
    "s_waitcnt vmcnt(7)\n"
    "v_mfma_f32_16x16x32_bf16 v[216:219], v[94:97], v[124:127], v[216:219]\n"
    "v_mfma_f32_16x16x32_bf16 v[220:223], v[92:95], v[124:127], v[220:223]\n"
    "s_waitcnt vmcnt(6)\n"
    "v_mfma_f32_16x16x32_bf16 v[224:227], v[94:97], v[132:135], v[224:227]\n"
    "v_mfma_f32_16x16x32_bf16 v[228:231], v[92:95], v[132:135], v[228:231]\n"
    "v_mfma_f32_16x16x32_bf16 v[228:231], a[124:127], a[236:239], v[228:231]\n"
    "v_mfma_f32_16x16x32_bf16 v[224:227], a[120:123], a[236:239], v[224:227]\n"
    "v_mfma_f32_16x16x32_bf16 v[224:227], a[112:115], a[232:235], v[224:227]\n"
    "v_mfma_f32_16x16x32_bf16 v[224:227], a[104:107], a[228:231], v[224:227]\n"
    "v_mfma_f32_16x16x32_bf16 v[224:227], a[96:99], a[224:227], v[224:227]\n"
    "s_barrier\n"
    "v_mfma_f32_16x16x32_bf16 v[200:203], a[96:99], a[240:243], v[200:203]\n"
    "s_waitcnt lgkmcnt(0)\n"
    "ds_read_b128 a[208:211], v29 offset:24576\n"
    "v_mfma_f32_16x16x32_bf16 v[200:203], a[104:107], a[244:247], v[200:203]\n"
    "ds_read_b128 a[212:215], v30 offset:24576\n"
    "v_mfma_f32_16x16x32_bf16 v[200:203], a[112:115], a[248:251], v[200:203]\n"
    "ds_read_b128 a[216:219], v31 offset:24576\n"
    "v_mfma_f32_16x16x32_bf16 v[200:203], a[120:123], a[252:255], v[200:203]\n"
    "ds_read_b128 a[220:223], v32 offset:24576\n"
    "v_mfma_f32_16x16x32_bf16 v[204:207], a[124:127], a[252:255], v[204:207]\n"
    "ds_read_b128 a[224:227], v29 offset:25600\n"
    "v_mfma_f32_16x16x32_bf16 v[204:207], a[116:119], a[248:251], v[204:207]\n"
    "ds_read_b128 a[228:231], v30 offset:25600\n"
    "v_mfma_f32_16x16x32_bf16 v[204:207], a[108:111], a[244:247], v[204:207]\n"
    "ds_read_b128 a[232:235], v31 offset:25600\n"
    "v_mfma_f32_16x16x32_bf16 v[204:207], a[100:103], a[240:243], v[204:207]\n"
    "ds_read_b128 a[236:239], v32 offset:25600\n"
    "v_mfma_f32_16x16x32_bf16 v[212:215], a[100:103], v[232:235], v[212:215]\n"
    "ds_read_b128 a[240:243], v21 offset:0\n"
    "v_mfma_f32_16x16x32_bf16 v[212:215], a[108:111], v[236:239], v[212:215]\n"
    "s_waitcnt vmcnt(4)\n"
    "ds_read_b128 a[244:247], v22 offset:0\n"
    "v_cmp_ne_u32 vcc, 1, v61\n"
    "v_mfma_f32_16x16x32_bf16 v[212:215], a[116:119], v[240:243], v[212:215]\n"
    "s_cbranch_vccnz .Lwf2_%=\n"
    ".Lokf2_%=:\n"
    "ds_read_b128 a[248:251], v23 offset:0\n"
    "s_add_u32 s62, s98, 32768\n"
    "v_mfma_f32_16x16x32_bf16 v[212:215], a[124:127], v[244:247], v[212:215]\n"
    "ds_read_b128 a[252:255], v24 offset:0\n"
    "buffer_load_dwordx4 v[172:175], v53, s[52:55], s62 offen offset:0 sc1\n"
    "v_mfma_f32_16x16x32_bf16 v[208:211], a[120:123], v[244:247], v[208:211]\n"
    "buffer_load_dwordx4 v[180:183], v53, s[52:55], s62 offen offset:1024 sc1\n"
    "v_mfma_f32_16x16x32_bf16 v[208:211], a[112:115], v[240:243], v[208:211]\n"
    "buffer_load_dwordx4 v[188:191], v53, s[52:55], s62 offen offset:2048 sc1\n"
    "v_mfma_f32_16x16x32_bf16 v[208:211], a[104:107], v[236:239], v[208:211]\n"
    "buffer_load_dwordx4 v[196:199], v53, s[52:55], s62 offen offset:3072 sc1\n"
    "v_mfma_f32_16x16x32_bf16 v[208:211], a[96:99], v[232:235], v[208:211]\n"
    "s_waitcnt lgkmcnt(3)\n"
    "ds_read_b128 v[232:235], v21 offset:1024\n"
    "v_mfma_f32_16x16x32_bf16 v[216:219], a[128:131], a[208:211], v[216:219]\n"
    "ds_read_b128 v[236:239], v22 offset:1024\n"
    "v_mfma_f32_16x16x32_bf16 v[216:219], a[136:139], a[212:215], v[216:219]\n"
    "ds_read_b128 v[240:243], v23 offset:1024\n"
    "v_mfma_f32_16x16x32_bf16 v[216:219], a[144:147], a[216:219], v[216:219]\n"
    "ds_read_b128 v[244:247], v24 offset:1024\n"
    "v_mfma_f32_16x16x32_bf16 v[216:219], a[152:155], a[220:223], v[216:219]\n"
    "v_mfma_f32_16x16x32_bf16 v[220:223], a[156:159], a[220:223], v[220:223]\n"
    "v_mfma_f32_16x16x32_bf16 v[220:223], a[148:151], a[216:219], v[220:223]\n"
    "v_mfma_f32_16x16x32_bf16 v[220:223], a[140:143], a[212:215], v[220:223]\n"
    "v_mfma_f32_16x16x32_bf16 v[220:223], a[132:135], a[208:211], v[220:223]\n"
    "v_mfma_f32_16x16x32_bf16 v[228:231], a[132:135], a[224:227], v[228:231]\n"
    "v_mfma_f32_16x16x32_bf16 v[228:231], a[140:143], a[228:231], v[228:231]\n"
    "v_mfma_f32_16x16x32_bf16 v[228:231], a[148:151], a[232:235], v[228:231]\n"
    "v_mfma_f32_16x16x32_bf16 v[228:231], a[156:159], a[236:239], v[228:231]\n"
    "v_mfma_f32_16x16x32_bf16 v[224:227], a[152:155], a[236:239], v[224:227]\n"
    "v_mfma_f32_16x16x32_bf16 v[224:227], a[144:147], a[232:235], v[224:227]\n"
    "v_mfma_f32_16x16x32_bf16 v[224:227], a[136:139], a[228:231], v[224:227]\n"
    "v_mfma_f32_16x16x32_bf16 v[224:227], a[128:131], a[224:227], v[224:227]\n"
    "v_mfma_f32_16x16x32_bf16 v[216:219], a[160:163], a[240:243], v[216:219]\n"
    "s_waitcnt vmcnt(7)\n"
    "v_mfma_f32_16x16x32_bf16 v[200:203], v[94:97], v[140:143], v[200:203]\n"
    "v_mfma_f32_16x16x32_bf16 v[204:207], v[92:95], v[140:143], v[204:207]\n"
    "s_waitcnt vmcnt(6)\n"
    "v_mfma_f32_16x16x32_bf16 v[208:211], v[94:97], v[148:151], v[208:211]\n"
    "v_mfma_f32_16x16x32_bf16 v[212:215], v[92:95], v[148:151], v[212:215]\n"
    "s_waitcnt vmcnt(5)\n"
    "v_mfma_f32_16x16x32_bf16 v[216:219], v[94:97], v[156:159], v[216:219]\n"
    "v_mfma_f32_16x16x32_bf16 v[220:223], v[92:95], v[156:159], v[220:223]\n"
    "s_waitcnt vmcnt(4)\n"
    "v_mfma_f32_16x16x32_bf16 v[224:227], v[94:97], v[164:167], v[224:227]\n"
    "v_mfma_f32_16x16x32_bf16 v[228:231], v[92:95], v[164:167], v[228:231]\n"
    "s_waitcnt lgkmcnt(0)\n"
    "v_mfma_f32_16x16x32_bf16 v[216:219], a[168:171], a[244:247], v[216:219]\n"
    "v_mfma_f32_16x16x32_bf16 v[216:219], a[176:179], a[248:251], v[216:219]\n"
    "v_mfma_f32_16x16x32_bf16 v[216:219], a[184:187], a[252:255], v[216:219]\n"
    "v_mfma_f32_16x16x32_bf16 v[220:223], a[188:191], a[252:255], v[220:223]\n"
    "v_mfma_f32_16x16x32_bf16 v[220:223], a[180:183], a[248:251], v[220:223]\n"
    "v_mfma_f32_16x16x32_bf16 v[220:223], a[172:175], a[244:247], v[220:223]\n"
    "v_mfma_f32_16x16x32_bf16 v[220:223], a[164:167], a[240:243], v[220:223]\n"
    "v_mfma_f32_16x16x32_bf16 v[228:231], a[164:167], v[232:235], v[228:231]\n"
    "v_mfma_f32_16x16x32_bf16 v[228:231], a[172:175], v[236:239], v[228:231]\n"
    "v_mfma_f32_16x16x32_bf16 v[228:231], a[180:183], v[240:243], v[228:231]\n"
    "v_mfma_f32_16x16x32_bf16 v[228:231], a[188:191], v[244:247], v[228:231]\n"
    "v_mfma_f32_16x16x32_bf16 v[224:227], a[184:187], v[244:247], v[224:227]\n"
    "v_mfma_f32_16x16x32_bf16 v[224:227], a[176:179], v[240:243], v[224:227]\n"
    "v_mfma_f32_16x16x32_bf16 v[224:227], a[168:171], v[236:239], v[224:227]\n"
    "v_mfma_f32_16x16x32_bf16 v[224:227], a[160:163], v[232:235], v[224:227]\n"
    "s_waitcnt vmcnt(3)\n"
    "v_mfma_f32_16x16x32_bf16 v[200:203], v[94:97], v[172:175], v[200:203]\n"
    "s_lshl2_add_u32 s62, s56, 16384\n"
    "v_lshlrev_b32 v74, 4, v1\n"
    "v_xor_b32 v75, s15, v1\n"
    "v_mfma_f32_16x16x32_bf16 v[204:207], v[92:95], v[172:175], v[204:207]\n"
    "s_lshl_b32 s63, s56, 8\n"
    "v_lshl_add_u32 v75, v75, 4, s62\n"
    "s_add_u32 s63, s63, 16384\n"
    "v_add_u32 v74, s63, v74\n"
    "s_waitcnt vmcnt(2)\n"
    "v_mfma_f32_16x16x32_bf16 v[208:211], v[94:97], v[180:183], v[208:211]\n"
    "v_cvt_pk_bf16_f32 v136, v200, v201\n"
    "v_cvt_pk_bf16_f32 v137, v202, v203\n"
    "v_mfma_f32_16x16x32_bf16 v[212:215], v[92:95], v[180:183], v[212:215]\n"
    "v_cvt_pk_bf16_f32 v138, v204, v205\n"
    "v_cvt_pk_bf16_f32 v139, v206, v207\n"
    "v_pk_max_i16 v136, v136, 0\n"
    "s_waitcnt vmcnt(1)\n"
    "v_mfma_f32_16x16x32_bf16 v[216:219], v[94:97], v[188:191], v[216:219]\n"
    "v_pk_max_i16 v137, v137, 0\n"
    "v_pk_max_i16 v138, v138, 0\n"
    "v_pk_max_i16 v139, v139, 0\n"
    "v_mfma_f32_16x16x32_bf16 v[220:223], v[92:95], v[188:191], v[220:223]\n"
    "v_cvt_pk_bf16_f32 v144, v208, v209\n"
    "v_cvt_pk_bf16_f32 v145, v210, v211\n"
    "v_cvt_pk_bf16_f32 v146, v212, v213\n"
    "s_waitcnt vmcnt(0)\n"
    "v_mfma_f32_16x16x32_bf16 v[224:227], v[94:97], v[196:199], v[224:227]\n"
    "v_cvt_pk_bf16_f32 v147, v214, v215\n"
    "v_pk_max_i16 v144, v144, 0\n"
    "v_pk_max_i16 v145, v145, 0\n"
    "v_mfma_f32_16x16x32_bf16 v[228:231], v[92:95], v[196:199], v[228:231]\n"
    "v_pk_max_i16 v146, v146, 0\n"
    "v_pk_max_i16 v147, v147, 0\n"
    "v_cvt_pk_bf16_f32 v152, v216, v217\n"
    "v_mfma_f32_16x16x32_bf16 v[100:103], v[104:107], v[136:139], v[100:103]\n"
    "v_cvt_pk_bf16_f32 v153, v218, v219\n"
    "v_cvt_pk_bf16_f32 v154, v220, v221\n"
    "v_cvt_pk_bf16_f32 v155, v222, v223\n"
    "v_mfma_f32_16x16x32_bf16 v[100:103], v[112:115], v[144:147], v[100:103]\n"
    "v_pk_max_i16 v152, v152, 0\n"
    "v_pk_max_i16 v153, v153, 0\n"
    "v_pk_max_i16 v154, v154, 0\n"
    "v_pk_max_i16 v155, v155, 0\n"
    "v_cvt_pk_bf16_f32 v160, v224, v225\n"
    "v_cvt_pk_bf16_f32 v161, v226, v227\n"
    "v_mfma_f32_16x16x32_bf16 v[100:103], v[120:123], v[152:155], v[100:103]\n"
    "v_cvt_pk_bf16_f32 v162, v228, v229\n"
    "v_cvt_pk_bf16_f32 v163, v230, v231\n"
    "v_pk_max_i16 v160, v160, 0\n"
    "v_pk_max_i16 v161, v161, 0\n"
    "v_pk_max_i16 v162, v162, 0\n"
    "v_pk_max_i16 v163, v163, 0\n"
    "s_nop 1\n"
    "v_mfma_f32_16x16x32_bf16 v[100:103], v[128:131], v[160:163], v[100:103]\n"
    "s_xor_b32 s62, s58, s56\n"
    "s_lshl_b32 s62, s62, 11\n"
    "s_xor_b32 s62, s62, s39\n"
    "v_lshl_add_u32 v62, v1, 3, s62\n"
    "s_lshl4_add_u32 s63, s56, s99\n"
    "v_add_lshl_u32 v63, v1, s63, 1\n"
    "s_nop 1\n"
    "ds_write_b32 v75, v100\n"
    "s_mov_b64 exec, 0xffff\n"
    "s_mov_b32 s63, 0x37800080\n"
    "s_waitcnt lgkmcnt(0)\n"
    "s_barrier\n"
    "ds_read_b128 v[68:71], v74\n"
    "s_waitcnt lgkmcnt(0)\n"
    "v_pk_add_f32 v[68:69], v[68:69], v[70:71]\n"
    "v_add_f32 v100, v68, v69\n"
    "v_med3_f32 v100, v100, -s101, s101\n"
    "v_fract_f32 v68, v100\n"
    "v_cvt_flr_i32_f32 v71, v100\n"
    "v_cvt_pknorm_u16_f32 v70, s63, v68\n"
    "global_atomic_add_x2 v[72:73], v62, v[70:71], s[18:19] sc0 sc1\n"
    "s_mov_b32 s62, 0x2f800000\n"
    "s_waitcnt vmcnt(0)\n"
    "v_add_co_u32 v70, vcc, v70, v72\n"
    "v_and_b32 v72, 15, v72\n"
    "v_cvt_f32_u32 v70, v70\n"
    "v_addc_co_u32 v71, vcc, v71, v73, vcc\n"
    "v_cmpx_eq_u32 exec, 3, v72\n"
    "v_cvt_f32_i32 v71, v71\n"
    "v_fma_f32 v100, v70, s62, v71\n"
    "v_mul_f32 v100, 0xbfb8aa3b, v100\n"
    "v_exp_f32 v100, v100\n"
    "s_nop 0\n"
    "v_add_f32 v100, 1.0, v100\n"
    "v_rcp_f32 v100, v100\n"
    "s_nop 0\n"
    "v_cvt_pk_bf16_f32 v100, v100, v100\n"
    "global_store_short v63, v100, s[32:33]\n"
    "s_mov_b64 exec, -1\n"
    ".Lend_%=:\n"
    "s_endpgm\n"
    ".Lwr_%=:\n"
    "s_add_u32 s100, s100, 1\n"
    "s_cmp_gt_u32 s100, 100000\n"
    "s_cbranch_scc1 .Lokr_%=\n"
    "buffer_store_dword v57, v54, s[52:55], 0 offen sc1\n"
    "buffer_load_dword v56, v55, s[52:55], s39 offen offset:128 sc1\n"
    "s_waitcnt vmcnt(0)\n"
    "v_cmp_ne_u32 vcc, 1, v56\n"
    "s_cbranch_vccnz .Lwr_%=\n"
    "s_branch .Lokr_%=\n"
    ".Lwf0_%=:\n"
    "s_add_u32 s100, s100, 1\n"
    "s_cmp_gt_u32 s100, 100000\n"
    "s_cbranch_scc1 .Lokf0_%=\n"
    "buffer_load_dword v59, off, s[52:55], s59 offset:192 sc1\n"
    "s_waitcnt vmcnt(0)\n"
    "v_cmp_ne_u32 vcc, 1, v59\n"
    "s_cbranch_vccnz .Lwf0_%=\n"
    "s_branch .Lokf0_%=\n"
    ".Lwf1_%=:\n"
    "s_add_u32 s100, s100, 1\n"
    "s_cmp_gt_u32 s100, 100000\n"
    "s_cbranch_scc1 .Lokf1_%=\n"
    "buffer_load_dword v60, off, s[52:55], s59 offset:208 sc1\n"
    "s_waitcnt vmcnt(0)\n"
    "v_cmp_ne_u32 vcc, 1, v60\n"
    "s_cbranch_vccnz .Lwf1_%=\n"
    "s_branch .Lokf1_%=\n"
    ".Lwf2_%=:\n"
    "s_add_u32 s100, s100, 1\n"
    "s_cmp_gt_u32 s100, 100000\n"
    "s_cbranch_scc1 .Lokf2_%=\n"
    "buffer_load_dword v61, off, s[52:55], s59 offset:224 sc1\n"
    "s_waitcnt vmcnt(0)\n"
    "v_cmp_ne_u32 vcc, 1, v61\n"
    "s_cbranch_vccnz .Lwf2_%=\n"
    "s_branch .Lokf2_%=\n"
    ".Lalt_%=:\n"
    ".Lbody_b%=:\n"
    "s_lshl_b32 s63, s56, 12\n"
    "v_lshl_add_u32 v53, v1, 4, s63\n"
    "s_mul_i32 s98, %1, 49152\n"
    "v_and_b32 v72, 1, v1\n"
    "v_lshl_add_u32 v72, s56, 1, v72\n"
    "s_cmp_lt_u32 s56, 2\n"
    "s_cselect_b32 s63, 1, 8\n"
    "v_and_b32 v73, 3, v72\n"
    "v_mul_u32_u24 v73, s63, v73\n"
    "v_lshlrev_b32 v73, 8, v73\n"
    "v_xor_b32 v73, s39, v73\n"
    "v_lshl_add_u32 v54, v72, 2, v73\n"
    "v_add_u32 v54, 128, v54\n"
    "v_mov_b32 v57, 1\n"
    "s_lshl2_add_u32 s62, s58, s56\n"
    "s_lshl_b32 s63, s62, 6\n"
    "v_lshl_add_u32 v65, v70, 3, s63\n"
    "s_lshl2_add_u32 s99, s38, s61\n"
    "s_lshl_b32 s99, s99, 6\n"
    "v_xor_b32 v64, s15, v1\n"
    "v_add_u32 v64, s99, v64\n"
    "v_mul_u32_u24 v64, 26, v64\n"
    "s_min_u32 s63, s62, 12\n"
    "s_lshl_b32 s63, s63, 1\n"
    "v_add_u32 v64, s63, v64\n"
    "s_add_u32 s26, s26, s63\n"
    "s_addc_u32 s27, s27, 0\n"
    "s_cmp_eq_u32 s62, 13\n"
    "s_cselect_b64 s[26:27], s[28:29], s[26:27]\n"
    "s_cmp_eq_u32 s62, 14\n"
    "s_cselect_b64 s[26:27], s[30:31], s[26:27]\n"
    "s_cmp_lt_u32 s62, 13\n"
    "s_cselect_b32 s16, -1, 0\n"
    "s_cselect_b32 s17, 0, 1.0\n"
    "s_cmp_lt_u32 s62, 15\n"
    "s_cselect_b32 s60, -1, 0\n"
    "v_lshrrev_b32 v68, 2, v71\n"
    "v_lshlrev_b32 v69, 11, v68\n"
    "v_and_b32 v73, 3, v1\n"
    "v_lshl_add_u32 v69, v73, 8, v69\n"
    "v_xor_b32 v72, v70, v71\n"
    "v_lshl_add_u32 v21, v72, 4, v69\n"
    "v_xor_b32 v22, 64, v21\n"
    "v_xor_b32 v23, 128, v21\n"
    "v_xor_b32 v24, 192, v21\n"
    "v_lshrrev_b32 v72, 1, v71\n"
    "v_xor_b32 v72, v70, v72\n"
    "v_lshl_add_u32 v69, v72, 4, v69\n"
    "v_and_b32 v72, 1, v1\n"
    "v_lshl_add_u32 v33, v72, 7, v69\n"
    "v_xor_b32 v34, 64, v33\n"
    "v_xor_b32 v35, 128, v33\n"
    "v_xor_b32 v36, 192, v33\n"
    "s_lshr_b32 s63, s56, 1\n"
    "s_lshl_b32 s63, s63, 12\n"
    "v_lshl_add_u32 v69, v70, 3, v68\n"
    "v_lshl_add_u32 v69, v69, 7, s63\n"
    "v_lshl_add_u32 v69, v73, 3, v69\n"
    "v_bfe_u32 v72, v1, 3, 2\n"
    "s_and_b32 s63, s56, 1\n"
    "s_lshl_b32 s63, s63, 1\n"
    "v_xor_b32 v72, s63, v72\n"
    "v_lshl_add_u32 v45, v72, 5, v69\n"
    "v_xor_b32 v46, 32, v45\n"
    "v_add_u32 v25, 65536, v21\n"
    "v_add_u32 v26, 65536, v22\n"
    "v_add_u32 v27, 65536, v23\n"
    "v_add_u32 v28, 65536, v24\n"
    "v_add_u32 v37, 65536, v33\n"
    "v_add_u32 v38, 65536, v34\n"
    "v_add_u32 v39, 65536, v35\n"
    "v_add_u32 v40, 65536, v36\n"
    "v_add_u32 v47, 65536, v45\n"
    "v_add_u32 v48, 65536, v46\n"
    "v_add_u32 v29, 131072, v21\n"
    "v_add_u32 v30, 131072, v22\n"
    "v_add_u32 v31, 131072, v23\n"
    "v_add_u32 v32, 131072, v24\n"
    "v_add_u32 v41, 131072, v33\n"
    "v_add_u32 v42, 131072, v34\n"
    "v_add_u32 v43, 131072, v35\n"
    "v_add_u32 v44, 131072, v36\n"
    "v_add_u32 v49, 131072, v45\n"
    "v_add_u32 v50, 131072, v46\n"
    "v_cmp_eq_u32 vcc, v68, v70\n"
    "v_mov_b32 v74, 0x3f80\n"
    "v_mov_b32 v75, 0\n"
    "v_lshlrev_b32 v73, 4, v73\n"
    "v_cndmask_b32 v74, 0, v74, vcc\n"
    "v_mov_b32 v92, 0\n"
    "v_mov_b32 v93, 0\n"
    "v_mov_b32 v96, 0\n"
    "v_mov_b32 v97, 0\n"
    "v_lshlrev_b64 v[94:95], v73, v[74:75]\n"
    "s_lshl2_add_u32 s59, s56, s39\n"
    "buffer_load_dword v9, v6, s[40:43], s34 offen\n"
    "buffer_load_dword v10, v7, s[40:43], s34 offen\n"
    "buffer_load_dword v11, v6, s[40:43], s35 offen\n"
    "buffer_load_dword v13, v6, s[40:43], s36 offen\n"
    "buffer_load_dword v12, v7, s[40:43], s35 offen\n"
    "s_add_u32 m0, s57, 32768\n"
    "v_readlane_b32 s96, v8, 3\n"
    "s_xor_b32 s96, s96, 0\n"
    "buffer_load_dwordx4 v51, s[44:47], s96 offen offset:0 lds\n"
    "buffer_load_dwordx4 v52, s[44:47], s96 offen offset:1024 lds\n"
    "s_add_u32 m0, s57, 40960\n"
    "v_readlane_b32 s96, v8, 2\n"
    "s_xor_b32 s96, s96, 0\n"
    "buffer_load_dwordx4 v51, s[44:47], s96 offen offset:0 lds\n"
    "s_waitcnt vmcnt(9)\n"
    "s_barrier\n"
    "ds_read_b64_tr_b16 a[0:1], v45 offset:0\n"
    "buffer_load_dwordx4 v52, s[44:47], s96 offen offset:1024 lds\n"
    "ds_read_b64_tr_b16 a[2:3], v45 offset:512\n"
    "ds_read_b64_tr_b16 a[8:9], v45 offset:8192\n"
    "ds_read_b64_tr_b16 a[10:11], v45 offset:8704\n"
    "s_add_u32 m0, s57, 49152\n"
    "s_xor_b32 s96, s96, 32768\n"
    "buffer_load_dwordx4 v51, s[44:47], s96 offen offset:0 lds\n"
    "ds_read_b64_tr_b16 a[16:17], v45 offset:16384\n"
    "ds_read_b64_tr_b16 a[18:19], v45 offset:16896\n"
    "ds_read_b64_tr_b16 a[24:25], v45 offset:24576\n"
    "buffer_load_dwordx4 v52, s[44:47], s96 offen offset:1024 lds\n"
    "ds_read_b64_tr_b16 a[26:27], v45 offset:25088\n"
    "ds_read_b64_tr_b16 a[4:5], v46 offset:0\n"
    "ds_read_b64_tr_b16 a[6:7], v46 offset:512\n"
    "s_add_u32 m0, s57, 57344\n"
    "s_xor_b32 s96, s96, 98304\n"
    "buffer_load_dwordx4 v51, s[44:47], s96 offen offset:0 lds\n"
    "ds_read_b64_tr_b16 a[12:13], v46 offset:8192\n"
    "ds_read_b64_tr_b16 a[14:15], v46 offset:8704\n"
    "ds_read_b64_tr_b16 a[20:21], v46 offset:16384\n"
    "buffer_load_dwordx4 v52, s[44:47], s96 offen offset:1024 lds\n"
    "ds_read_b64_tr_b16 a[22:23], v46 offset:16896\n"
    "ds_read_b64_tr_b16 a[28:29], v46 offset:24576\n"
    "ds_read_b64_tr_b16 a[30:31], v46 offset:25088\n"
    "s_add_u32 m0, s57, 65536\n"
    "s_xor_b32 s96, s96, 32768\n"
    "buffer_load_dwordx4 v51, s[44:47], s96 offen offset:0 lds\n"
    "buffer_load_dwordx4 v52, s[44:47], s96 offen offset:1024 lds\n"
    "s_add_u32 m0, s57, 73728\n"
    "v_readlane_b32 s96, v8, 4\n"
    "s_xor_b32 s96, s96, 0\n"
    "buffer_load_dwordx4 v51, s[44:47], s96 offen offset:0 lds\n"
    "buffer_load_dwordx4 v52, s[44:47], s96 offen offset:1024 lds\n"
    "s_add_u32 m0, s57, 81920\n"
    "v_readlane_b32 s96, v8, 5\n"
    "s_xor_b32 s96, s96, 0\n"
    "buffer_load_dwordx4 v51, s[44:47], s96 offen offset:0 lds\n"
    "buffer_load_dwordx4 v52, s[44:47], s96 offen offset:1024 lds\n"
    "s_add_u32 m0, s57, 90112\n"
    "v_readlane_b32 s96, v8, 3\n"
    "s_xor_b32 s96, s96, 32768\n"
    "buffer_load_dwordx4 v51, s[44:47], s96 offen offset:0 lds\n"
    "buffer_load_dwordx4 v52, s[44:47], s96 offen offset:1024 lds\n"
    "s_waitcnt vmcnt(8) lgkmcnt(0)\n"
    "s_barrier\n"
    "v_lshlrev_b32 v9, 8, v9\n"
    "v_lshlrev_b32 v10, 8, v10\n"
    "v_lshlrev_b32 v11, 8, v11\n"
    "v_lshlrev_b32 v12, 8, v12\n"
    "v_lshlrev_b32 v13, 8, v13\n"
    "ds_read_b64_tr_b16 a[96:97], v45 offset:32768\n"
    "s_add_u32 m0, s57, 98304\n"
    "v_readlane_b32 s96, v8, 4\n"
    "s_xor_b32 s96, s96, 32768\n"
    "buffer_load_dwordx4 v51, s[44:47], s96 offen offset:0 lds\n"
    "ds_read_b64_tr_b16 a[98:99], v45 offset:33280\n"
    "ds_read_b64_tr_b16 a[100:101], v46 offset:32768\n"
    "ds_read_b64_tr_b16 a[102:103], v46 offset:33280\n"
    "ds_read_b64_tr_b16 a[64:65], v45 offset:40960\n"
    "buffer_load_dwordx4 v52, s[44:47], s96 offen offset:1024 lds\n"
    "ds_read_b64_tr_b16 a[66:67], v45 offset:41472\n"
    "ds_read_b64_tr_b16 a[72:73], v45 offset:49152\n"
    "ds_read_b64_tr_b16 a[74:75], v45 offset:49664\n"
    "ds_read_b64_tr_b16 a[80:81], v45 offset:57344\n"
    "ds_read_b64_tr_b16 a[82:83], v45 offset:57856\n"
    "s_add_u32 m0, s57, 106496\n"
    "v_readlane_b32 s96, v8, 5\n"
    "s_xor_b32 s96, s96, 32768\n"
    "buffer_load_dwordx4 v51, s[44:47], s96 offen offset:0 lds\n"
    "ds_read_b64_tr_b16 a[68:69], v46 offset:40960\n"
    "ds_read_b64_tr_b16 a[70:71], v46 offset:41472\n"
    "ds_read_b64_tr_b16 a[76:77], v46 offset:49152\n"
    "ds_read_b64_tr_b16 a[78:79], v46 offset:49664\n"
    "ds_read_b64_tr_b16 a[84:85], v46 offset:57344\n"
    "buffer_load_dwordx4 v52, s[44:47], s96 offen offset:1024 lds\n"
    "ds_read_b64_tr_b16 a[86:87], v46 offset:57856\n"
    "s_add_u32 m0, s57, 114688\n"
    "v_readlane_b32 s96, v8, 1\n"
    "s_xor_b32 s96, s96, 0\n"
    "buffer_load_dwordx4 v51, s[44:47], s96 offen offset:0 lds\n"
    "buffer_load_dwordx4 v52, s[44:47], s96 offen offset:1024 lds\n"
    "s_add_u32 m0, s57, 122880\n"
    "s_xor_b32 s96, s96, 32768\n"
    "buffer_load_dwordx4 v51, s[44:47], s96 offen offset:0 lds\n"
    "buffer_load_dwordx4 v52, s[44:47], s96 offen offset:1024 lds\n"
    "s_waitcnt vmcnt(8) lgkmcnt(0)\n"
    "s_barrier\n"
    "ds_read_b64_tr_b16 a[104:105], v47 offset:24576\n"
    "ds_read_b64_tr_b16 a[106:107], v47 offset:25088\n"
    "ds_read_b64_tr_b16 a[108:109], v48 offset:24576\n"
    "s_add_u32 m0, s57, 131072\n"
    "v_readlane_b32 s96, v8, 3\n"
    "s_xor_b32 s96, s96, 65536\n"
    "buffer_load_dwordx4 v51, s[44:47], s96 offen offset:0 lds\n"
    "ds_read_b64_tr_b16 a[110:111], v48 offset:25088\n"
    "ds_read_b64_tr_b16 a[88:89], v47 offset:0\n"
    "ds_read_b64_tr_b16 a[90:91], v47 offset:512\n"
    "ds_read_b64_tr_b16 a[92:93], v48 offset:0\n"
    "buffer_load_dwordx4 v52, s[44:47], s96 offen offset:1024 lds\n"
    "ds_read_b64_tr_b16 a[94:95], v48 offset:512\n"
    "ds_read_b64_tr_b16 a[128:129], v47 offset:8192\n"
    "ds_read_b64_tr_b16 a[130:131], v47 offset:8704\n"
    "ds_read_b64_tr_b16 a[132:133], v48 offset:8192\n"
    "ds_read_b64_tr_b16 a[134:135], v48 offset:8704\n"
    "ds_read_b64_tr_b16 a[160:161], v47 offset:16384\n"
    "s_add_u32 m0, s57, 139264\n"
    "v_readlane_b32 s96, v8, 4\n"
    "s_xor_b32 s96, s96, 65536\n"
    "buffer_load_dwordx4 v51, s[44:47], s96 offen offset:0 lds\n"
    "ds_read_b64_tr_b16 a[162:163], v47 offset:16896\n"
    "ds_read_b64_tr_b16 a[164:165], v48 offset:16384\n"
    "ds_read_b64_tr_b16 a[166:167], v48 offset:16896\n"
    "buffer_load_dwordx4 v52, s[44:47], s96 offen offset:1024 lds\n"
    "s_add_u32 m0, s57, 147456\n"
    "v_readlane_b32 s96, v8, 3\n"
    "s_xor_b32 s96, s96, 98304\n"
    "buffer_load_dwordx4 v51, s[44:47], s96 offen offset:0 lds\n"
    "buffer_load_dwordx4 v52, s[44:47], s96 offen offset:1024 lds\n"
    "s_add_u32 m0, s57, 155648\n"
    "v_readlane_b32 s96, v8, 4\n"
    "s_xor_b32 s96, s96, 98304\n"
    "buffer_load_dwordx4 v51, s[44:47], s96 offen offset:0 lds\n"
    "buffer_load_dwordx4 v52, s[44:47], s96 offen offset:1024 lds\n"
    "s_waitcnt vmcnt(8) lgkmcnt(0)\n"
    "s_barrier\n"
    "ds_read_b64_tr_b16 a[136:137], v47 offset:32768\n"
    "ds_read_b64_tr_b16 a[138:139], v47 offset:33280\n"
    "ds_read_b64_tr_b16 a[140:141], v48 offset:32768\n"
    "s_add_u32 m0, s57, 0\n"
    "v_readlane_b32 s96, v8, 5\n"
    "s_xor_b32 s96, s96, 65536\n"
    "buffer_load_dwordx4 v51, s[44:47], s96 offen offset:0 lds\n"
    "ds_read_b64_tr_b16 a[142:143], v48 offset:33280\n"
    "ds_read_b64_tr_b16 a[168:169], v47 offset:40960\n"
    "ds_read_b64_tr_b16 a[170:171], v47 offset:41472\n"
    "ds_read_b64_tr_b16 a[172:173], v48 offset:40960\n"
    "buffer_load_dwordx4 v52, s[44:47], s96 offen offset:1024 lds\n"
    "ds_read_b64_tr_b16 a[174:175], v48 offset:41472\n"
    "ds_read_b64_tr_b16 a[32:33], v47 offset:49152\n"
    "ds_read_b64_tr_b16 a[34:35], v47 offset:49664\n"
    "ds_read_b64_tr_b16 a[40:41], v47 offset:57344\n"
    "ds_read_b64_tr_b16 a[42:43], v47 offset:57856\n"
    "ds_read_b64_tr_b16 a[36:37], v48 offset:49152\n"
    "s_add_u32 m0, s57, 8192\n"
    "v_add_u32_dpp v17, v10, v2 row_newbcast:3 row_mask:0xf bank_mask:0xf\n"
    "buffer_load_dwordx4 v17, s[48:51], 0 offen offset:0 lds\n"
    "ds_read_b64_tr_b16 a[38:39], v48 offset:49664\n"
    "ds_read_b64_tr_b16 a[44:45], v48 offset:57344\n"
    "ds_read_b64_tr_b16 a[46:47], v48 offset:57856\n"
    "v_add_u32_dpp v18, v10, v3 row_newbcast:11 row_mask:0xf bank_mask:0xf\n"
    "buffer_load_dwordx4 v18, s[48:51], 0 offen offset:1024 lds\n"
    "s_add_u32 m0, s57, 16384\n"
    "v_add_u32_dpp v19, v9, v2 row_newbcast:2 row_mask:0xf bank_mask:0xf\n"
    "buffer_load_dwordx4 v19, s[48:51], 0 offen offset:0 lds\n"
    "v_add_u32_dpp v20, v9, v3 row_newbcast:10 row_mask:0xf bank_mask:0xf\n"
    "buffer_load_dwordx4 v20, s[48:51], 0 offen offset:1024 lds\n"
    "s_add_u32 m0, s57, 24576\n"
    "v_add_u32_dpp v17, v11, v2 row_newbcast:2 row_mask:0xf bank_mask:0xf\n"
    "buffer_load_dwordx4 v17, s[48:51], 0 offen offset:0 lds\n"
    "v_add_u32_dpp v18, v11, v3 row_newbcast:10 row_mask:0xf bank_mask:0xf\n"
    "buffer_load_dwordx4 v18, s[48:51], 0 offen offset:1024 lds\n"
    "s_waitcnt vmcnt(8) lgkmcnt(0)\n"
    "s_barrier\n"
    "ds_read_b64_tr_b16 a[112:113], v49 offset:0\n"
    "ds_read_b64_tr_b16 a[114:115], v49 offset:512\n"
    "ds_read_b64_tr_b16 a[120:121], v49 offset:16384\n"
    "s_add_u32 m0, s57, 32768\n"
    "v_add_u32_dpp v19, v10, v2 row_newbcast:2 row_mask:0xf bank_mask:0xf\n"
    "buffer_load_dwordx4 v19, s[48:51], 0 offen offset:0 lds\n"
    "ds_read_b64_tr_b16 a[122:123], v49 offset:16896\n"
    "ds_read_b64_tr_b16 a[116:117], v50 offset:0\n"
    "ds_read_b64_tr_b16 a[118:119], v50 offset:512\n"
    "ds_read_b64_tr_b16 a[124:125], v50 offset:16384\n"
    "ds_read_b64_tr_b16 a[126:127], v50 offset:16896\n"
    "v_add_u32_dpp v20, v10, v3 row_newbcast:10 row_mask:0xf bank_mask:0xf\n"
    "buffer_load_dwordx4 v20, s[48:51], 0 offen offset:1024 lds\n"
    "ds_read_b64_tr_b16 a[144:145], v49 offset:8192\n"
    "ds_read_b64_tr_b16 a[146:147], v49 offset:8704\n"
    "ds_read_b64_tr_b16 a[152:153], v49 offset:24576\n"
    "ds_read_b64_tr_b16 a[154:155], v49 offset:25088\n"
    "ds_read_b64_tr_b16 a[148:149], v50 offset:8192\n"
    "s_add_u32 m0, s57, 40960\n"
    "v_add_u32_dpp v17, v9, v2 row_newbcast:3 row_mask:0xf bank_mask:0xf\n"
    "buffer_load_dwordx4 v17, s[48:51], 0 offen offset:0 lds\n"
    "ds_read_b64_tr_b16 a[150:151], v50 offset:8704\n"
    "ds_read_b64_tr_b16 a[156:157], v50 offset:24576\n"
    "ds_read_b64_tr_b16 a[158:159], v50 offset:25088\n"
    "s_waitcnt vmcnt(9) lgkmcnt(0)\n"
    "s_barrier\n"
    "v_add_u32_dpp v18, v9, v3 row_newbcast:11 row_mask:0xf bank_mask:0xf\n"
    "buffer_load_dwordx4 v18, s[48:51], 0 offen offset:1024 lds\n"
    "ds_read_b64_tr_b16 a[176:177], v45 offset:0\n"
    "ds_read_b64_tr_b16 a[178:179], v45 offset:512\n"
    "ds_read_b64_tr_b16 a[180:181], v46 offset:0\n"
    "buffer_load_dword v14, v7, s[40:43], s36 offen\n"
    "ds_read_b64_tr_b16 a[182:183], v46 offset:512\n"
    "buffer_store_dword v57, v54, s[52:55], 0 offen sc1\n"
    "s_add_u32 m0, s57, 49152\n"
    "v_add_u32_dpp v19, v12, v2 row_newbcast:3 row_mask:0xf bank_mask:0xf\n"
    "buffer_load_dwordx4 v19, s[48:51], 0 offen offset:0 lds\n"
    "s_waitcnt vmcnt(9) lgkmcnt(0)\n"
    "s_barrier\n"
    "ds_read_b128 a[208:211], v21 offset:8192\n"
    "ds_read_b128 a[212:215], v22 offset:8192\n"
    "s_waitcnt lgkmcnt(1)\n"
    "v_mfma_f32_16x16x32_bf16 v[120:123], a[96:99], a[208:211], 0\n"
    "s_waitcnt lgkmcnt(0)\n"
    "ds_read_b128 a[216:219], v23 offset:8192\n"
    "ds_read_b128 a[220:223], v24 offset:8192\n"
    "v_mfma_f32_16x16x32_bf16 v[120:123], a[104:107], a[212:215], v[120:123]\n"
    "s_waitcnt lgkmcnt(1)\n"
    "ds_read_b128 a[224:227], v21 offset:9216\n"
    "ds_read_b128 a[228:231], v22 offset:9216\n"
    "v_mfma_f32_16x16x32_bf16 v[120:123], a[112:115], a[216:219], v[120:123]\n"
    "s_waitcnt lgkmcnt(2)\n"
    "ds_read_b128 a[232:235], v23 offset:9216\n"
    "ds_read_b128 a[236:239], v24 offset:9216\n"
    "v_mfma_f32_16x16x32_bf16 v[120:123], a[120:123], a[220:223], v[120:123]\n"
    "ds_read_b128 a[240:243], v21 offset:16384\n"
    "v_add_u32_dpp v20, v12, v3 row_newbcast:11 row_mask:0xf bank_mask:0xf\n"
    "v_mfma_f32_16x16x32_bf16 v[124:127], a[124:127], a[220:223], 0\n"
    "ds_read_b128 a[244:247], v22 offset:16384\n"
    "buffer_load_dwordx4 v20, s[48:51], 0 offen offset:1024 lds\n"
    "v_mfma_f32_16x16x32_bf16 v[124:127], a[116:119], a[216:219], v[124:127]\n"
    "s_add_u32 m0, s57, 57344\n"
    "ds_read_b128 a[248:251], v23 offset:16384\n"
    "s_xor_b32 s96, s96, 32768\n"
    "v_mfma_f32_16x16x32_bf16 v[124:127], a[108:111], a[212:215], v[124:127]\n"
    "ds_read_b128 a[252:255], v24 offset:16384\n"
    "buffer_load_dwordx4 v51, s[44:47], s96 offen offset:0 lds\n"
    "v_mfma_f32_16x16x32_bf16 v[124:127], a[100:103], a[208:211], v[124:127]\n"
    "s_waitcnt lgkmcnt(7)\n"
    "ds_read_b128 v[232:235], v21 offset:17408\n"
    "v_mfma_f32_16x16x32_bf16 v[132:135], a[100:103], a[224:227], 0\n"
    "s_waitcnt lgkmcnt(7)\n"
    "ds_read_b128 v[236:239], v22 offset:17408\n"
    "buffer_load_dwordx4 v52, s[44:47], s96 offen offset:1024 lds\n"
    "v_mfma_f32_16x16x32_bf16 v[132:135], a[108:111], a[228:231], v[132:135]\n"
    "s_waitcnt lgkmcnt(6)\n"
    "ds_read_b128 v[240:243], v23 offset:17408\n"
    "s_add_u32 m0, s57, 90112\n"
    "v_mfma_f32_16x16x32_bf16 v[132:135], a[116:119], a[232:235], v[132:135]\n"
    "ds_read_b128 v[244:247], v24 offset:17408\n"
    "v_add_u32_dpp v17, v12, v2 row_newbcast:2 row_mask:0xf bank_mask:0xf\n"
    "v_mfma_f32_16x16x32_bf16 v[132:135], a[124:127], a[236:239], v[132:135]\n"
    "v_mfma_f32_16x16x32_bf16 v[128:131], a[120:123], a[236:239], 0\n"
    "v_mfma_f32_16x16x32_bf16 v[128:131], a[112:115], a[232:235], v[128:131]\n"
    "buffer_load_dwordx4 v17, s[48:51], 0 offen offset:0 lds\n"
    "v_add_u32_dpp v18, v12, v3 row_newbcast:10 row_mask:0xf bank_mask:0xf\n"
    "v_mfma_f32_16x16x32_bf16 v[128:131], a[104:107], a[228:231], v[128:131]\n"
    "v_mfma_f32_16x16x32_bf16 v[128:131], a[96:99], a[224:227], v[128:131]\n"
    "buffer_load_dwordx4 v18, s[48:51], 0 offen offset:1024 lds\n"
    "s_add_u32 m0, s57, 65536\n"
    "v_add_u32_dpp v19, v11, v2 row_newbcast:3 row_mask:0xf bank_mask:0xf\n"
    "buffer_load_dwordx4 v19, s[48:51], 0 offen offset:0 lds\n"
    "s_waitcnt vmcnt(9) lgkmcnt(0)\n"
    "s_barrier\n"
    "v_mfma_f32_16x16x32_bf16 v[104:107], a[64:67], a[240:243], 0\n"
    "ds_read_b128 a[208:211], v21 offset:32768\n"
    "v_add_u32_dpp v20, v11, v3 row_newbcast:11 row_mask:0xf bank_mask:0xf\n"
    "v_mfma_f32_16x16x32_bf16 v[104:107], a[72:75], a[244:247], v[104:107]\n"
    "ds_read_b128 a[212:215], v22 offset:32768\n"
    "buffer_load_dwordx4 v20, s[48:51], 0 offen offset:1024 lds\n"
    "v_mfma_f32_16x16x32_bf16 v[104:107], a[80:83], a[248:251], v[104:107]\n"
    "s_add_u32 m0, s57, 73728\n"
    "ds_read_b128 a[216:219], v23 offset:32768\n"
    "v_readlane_b32 s96, v8, 6\n"
    "v_mfma_f32_16x16x32_bf16 v[104:107], a[88:91], a[252:255], v[104:107]\n"
    "s_xor_b32 s96, s96, 0\n"
    "ds_read_b128 a[220:223], v24 offset:32768\n"
    "v_mfma_f32_16x16x32_bf16 v[108:111], a[92:95], a[252:255], 0\n"
    "ds_read_b128 a[224:227], v21 offset:33792\n"
    "buffer_load_dwordx4 v51, s[44:47], s96 offen offset:0 lds\n"
    "v_mfma_f32_16x16x32_bf16 v[108:111], a[84:87], a[248:251], v[108:111]\n"
    "ds_read_b128 a[228:231], v22 offset:33792\n"
    "v_mfma_f32_16x16x32_bf16 v[108:111], a[76:79], a[244:247], v[108:111]\n"
    "ds_read_b128 a[232:235], v23 offset:33792\n"
    "v_mfma_f32_16x16x32_bf16 v[108:111], a[68:71], a[240:243], v[108:111]\n"
    "ds_read_b128 a[236:239], v24 offset:33792\n"
    "v_mfma_f32_16x16x32_bf16 v[116:119], a[68:71], v[232:235], 0\n"
    "ds_read_b128 a[240:243], v21 offset:40960\n"
    "buffer_load_dwordx4 v52, s[44:47], s96 offen offset:1024 lds\n"
    "v_mfma_f32_16x16x32_bf16 v[116:119], a[76:79], v[236:239], v[116:119]\n"
    "s_add_u32 m0, s57, 81920\n"
    "ds_read_b128 a[244:247], v22 offset:40960\n"
    "v_add_u32_dpp v17, v13, v2 row_newbcast:2 row_mask:0xf bank_mask:0xf\n"
    "v_mfma_f32_16x16x32_bf16 v[116:119], a[84:87], v[240:243], v[116:119]\n"
    "ds_read_b128 a[248:251], v23 offset:40960\n"
    "v_mfma_f32_16x16x32_bf16 v[116:119], a[92:95], v[244:247], v[116:119]\n"
    "ds_read_b128 a[252:255], v24 offset:40960\n"
    "v_mfma_f32_16x16x32_bf16 v[112:115], a[88:91], v[244:247], 0\n"
    "v_mfma_f32_16x16x32_bf16 v[112:115], a[80:83], v[240:243], v[112:115]\n"
    "buffer_load_dwordx4 v17, s[48:51], 0 offen offset:0 lds\n"
    "v_add_u32_dpp v18, v13, v3 row_newbcast:10 row_mask:0xf bank_mask:0xf\n"
    "v_mfma_f32_16x16x32_bf16 v[112:115], a[72:75], v[236:239], v[112:115]\n"
    "v_mfma_f32_16x16x32_bf16 v[112:115], a[64:67], v[232:235], v[112:115]\n"
    "s_waitcnt lgkmcnt(3)\n"
    "ds_read_b128 v[232:235], v21 offset:41984\n"
    "v_mfma_f32_16x16x32_bf16 v[120:123], a[64:67], a[208:211], v[120:123]\n"
    "ds_read_b128 v[236:239], v22 offset:41984\n"
    "v_mfma_f32_16x16x32_bf16 v[120:123], a[72:75], a[212:215], v[120:123]\n"
    "ds_read_b128 v[240:243], v23 offset:41984\n"
    "buffer_load_dwordx4 v18, s[48:51], 0 offen offset:1024 lds\n"
    "v_mfma_f32_16x16x32_bf16 v[120:123], a[80:83], a[216:219], v[120:123]\n"
    "s_add_u32 m0, s57, 98304\n"
    "ds_read_b128 v[244:247], v24 offset:41984\n"
    "s_xor_b32 s96, s96, 32768\n"
    "v_mfma_f32_16x16x32_bf16 v[120:123], a[88:91], a[220:223], v[120:123]\n"
    "v_mfma_f32_16x16x32_bf16 v[124:127], a[92:95], a[220:223], v[124:127]\n"
    "v_mfma_f32_16x16x32_bf16 v[124:127], a[84:87], a[216:219], v[124:127]\n"
    "buffer_load_dwordx4 v51, s[44:47], s96 offen offset:0 lds\n"
    "v_mfma_f32_16x16x32_bf16 v[124:127], a[76:79], a[212:215], v[124:127]\n"
    "v_mfma_f32_16x16x32_bf16 v[124:127], a[68:71], a[208:211], v[124:127]\n"
    "v_mfma_f32_16x16x32_bf16 v[132:135], a[68:71], a[224:227], v[132:135]\n"
    "v_mfma_f32_16x16x32_bf16 v[132:135], a[76:79], a[228:231], v[132:135]\n"
    "v_mfma_f32_16x16x32_bf16 v[132:135], a[84:87], a[232:235], v[132:135]\n"
    "buffer_load_dwordx4 v52, s[44:47], s96 offen offset:1024 lds\n"
    "s_add_u32 m0, s57, 106496\n"
    "v_mfma_f32_16x16x32_bf16 v[132:135], a[92:95], a[236:239], v[132:135]\n"
    "v_readlane_b32 s96, v8, 1\n"
    "v_mfma_f32_16x16x32_bf16 v[128:131], a[88:91], a[236:239], v[128:131]\n"
    "s_xor_b32 s96, s96, 65536\n"
    "v_mfma_f32_16x16x32_bf16 v[128:131], a[80:83], a[232:235], v[128:131]\n"
    "v_mfma_f32_16x16x32_bf16 v[128:131], a[72:75], a[228:231], v[128:131]\n"
    "buffer_load_dwordx4 v51, s[44:47], s96 offen offset:0 lds\n"
    "v_mfma_f32_16x16x32_bf16 v[128:131], a[64:67], a[224:227], v[128:131]\n"
    "s_waitcnt vmcnt(11) lgkmcnt(0)\n"
    "s_barrier\n"
    "v_mfma_f32_16x16x32_bf16 v[104:107], a[96:99], a[240:243], v[104:107]\n"
    "ds_read_b128 a[208:211], v21 offset:24576\n"
    "buffer_load_dwordx4 v52, s[44:47], s96 offen offset:1024 lds\n"
    "v_mfma_f32_16x16x32_bf16 v[104:107], a[104:107], a[244:247], v[104:107]\n"
    "s_add_u32 m0, s57, 114688\n"
    "ds_read_b128 a[212:215], v22 offset:24576\n"
    "v_lshlrev_b32 v14, 8, v14\n"
    "s_nop 1\n"
    "v_add_u32_dpp v19, v14, v2 row_newbcast:3 row_mask:0xf bank_mask:0xf\n"
    "v_mfma_f32_16x16x32_bf16 v[104:107], a[112:115], a[248:251], v[104:107]\n"
    "ds_read_b128 a[216:219], v23 offset:24576\n"
    "v_mfma_f32_16x16x32_bf16 v[104:107], a[120:123], a[252:255], v[104:107]\n"
    "ds_read_b128 a[220:223], v24 offset:24576\n"
    "v_mfma_f32_16x16x32_bf16 v[108:111], a[124:127], a[252:255], v[108:111]\n"
    "ds_read_b128 a[224:227], v21 offset:25600\n"
    "buffer_load_dwordx4 v19, s[48:51], 0 offen offset:0 lds\n"
    "v_mfma_f32_16x16x32_bf16 v[108:111], a[116:119], a[248:251], v[108:111]\n"
    "ds_read_b128 a[228:231], v22 offset:25600\n"
    "v_add_u32_dpp v20, v14, v3 row_newbcast:11 row_mask:0xf bank_mask:0xf\n"
    "v_mfma_f32_16x16x32_bf16 v[108:111], a[108:111], a[244:247], v[108:111]\n"
    "ds_read_b128 a[232:235], v23 offset:25600\n"
    "v_mfma_f32_16x16x32_bf16 v[108:111], a[100:103], a[240:243], v[108:111]\n"
    "ds_read_b128 a[236:239], v24 offset:25600\n"
    "v_mfma_f32_16x16x32_bf16 v[116:119], a[100:103], v[232:235], v[116:119]\n"
    "ds_read_b128 a[240:243], v21 offset:49152\n"
    "buffer_load_dwordx4 v20, s[48:51], 0 offen offset:1024 lds\n"
    "v_mfma_f32_16x16x32_bf16 v[116:119], a[108:111], v[236:239], v[116:119]\n"
    "s_add_u32 m0, s57, 122880\n"
    "ds_read_b128 a[244:247], v22 offset:49152\n"
    "v_add_u32_dpp v17, v14, v2 row_newbcast:2 row_mask:0xf bank_mask:0xf\n"
    "v_mfma_f32_16x16x32_bf16 v[116:119], a[116:119], v[240:243], v[116:119]\n"
    "ds_read_b128 a[248:251], v23 offset:49152\n"
    "v_mfma_f32_16x16x32_bf16 v[116:119], a[124:127], v[244:247], v[116:119]\n"
    "ds_read_b128 a[252:255], v24 offset:49152\n"
    "v_mfma_f32_16x16x32_bf16 v[112:115], a[120:123], v[244:247], v[112:115]\n"
    "ds_read_b64_tr_b16 a[184:185], v45 offset:57344\n"
    "v_mfma_f32_16x16x32_bf16 v[112:115], a[112:115], v[240:243], v[112:115]\n"
    "ds_read_b64_tr_b16 a[186:187], v45 offset:57856\n"
    "buffer_load_dwordx4 v17, s[48:51], 0 offen offset:0 lds\n"
    "v_mfma_f32_16x16x32_bf16 v[112:115], a[104:107], v[236:239], v[112:115]\n"
    "ds_read_b64_tr_b16 a[188:189], v46 offset:57344\n"
    "v_add_u32_dpp v18, v14, v3 row_newbcast:10 row_mask:0xf bank_mask:0xf\n"
    "v_mfma_f32_16x16x32_bf16 v[112:115], a[96:99], v[232:235], v[112:115]\n"
    "s_waitcnt lgkmcnt(6)\n"
    "ds_read_b128 v[232:235], v21 offset:50176\n"
    "v_mfma_f32_16x16x32_bf16 v[136:139], a[64:67], a[208:211], 0\n"
    "ds_read_b128 v[236:239], v22 offset:50176\n"
    "v_mfma_f32_16x16x32_bf16 v[136:139], a[72:75], a[212:215], v[136:139]\n"
    "ds_read_b128 v[240:243], v23 offset:50176\n"
    "buffer_load_dwordx4 v18, s[48:51], 0 offen offset:1024 lds\n"
    "v_mfma_f32_16x16x32_bf16 v[136:139], a[80:83], a[216:219], v[136:139]\n"
    "s_add_u32 m0, s57, 131072\n"
    "ds_read_b128 v[244:247], v24 offset:50176\n"
    "v_add_u32_dpp v19, v13, v2 row_newbcast:3 row_mask:0xf bank_mask:0xf\n"
    "v_mfma_f32_16x16x32_bf16 v[136:139], a[88:91], a[220:223], v[136:139]\n"
    "ds_read_b64_tr_b16 a[190:191], v46 offset:57856\n"
    "v_mfma_f32_16x16x32_bf16 v[140:143], a[92:95], a[220:223], 0\n"
    "v_mfma_f32_16x16x32_bf16 v[140:143], a[84:87], a[216:219], v[140:143]\n"
    "buffer_load_dwordx4 v19, s[48:51], 0 offen offset:0 lds\n"
    "v_add_u32_dpp v20, v13, v3 row_newbcast:11 row_mask:0xf bank_mask:0xf\n"
    "v_mfma_f32_16x16x32_bf16 v[140:143], a[76:79], a[212:215], v[140:143]\n"
    "v_mfma_f32_16x16x32_bf16 v[140:143], a[68:71], a[208:211], v[140:143]\n"
    "v_mfma_f32_16x16x32_bf16 v[148:151], a[68:71], a[224:227], 0\n"
    "v_mfma_f32_16x16x32_bf16 v[148:151], a[76:79], a[228:231], v[148:151]\n"
    "v_mfma_f32_16x16x32_bf16 v[148:151], a[84:87], a[232:235], v[148:151]\n"
    "buffer_load_dwordx4 v20, s[48:51], 0 offen offset:1024 lds\n"
    "s_add_u32 m0, s57, 147456\n"
    "v_mfma_f32_16x16x32_bf16 v[148:151], a[92:95], a[236:239], v[148:151]\n"
    "v_add_u32_dpp v17, v9, v2 row_newbcast:0 row_mask:0xf bank_mask:0xf\n"
    "v_mfma_f32_16x16x32_bf16 v[144:147], a[88:91], a[236:239], 0\n"
    "v_mfma_f32_16x16x32_bf16 v[144:147], a[80:83], a[232:235], v[144:147]\n"
    "v_mfma_f32_16x16x32_bf16 v[144:147], a[72:75], a[228:231], v[144:147]\n"
    "buffer_load_dwordx4 v17, s[48:51], 0 offen offset:0 lds\n"
    "v_add_u32_dpp v18, v9, v3 row_newbcast:8 row_mask:0xf bank_mask:0xf\n"
    "v_mfma_f32_16x16x32_bf16 v[144:147], a[64:67], a[224:227], v[144:147]\n"
    "s_waitcnt vmcnt(13) lgkmcnt(0)\n"
    "s_barrier\n"
    "v_mfma_f32_16x16x32_bf16 v[152:155], a[96:99], a[240:243], 0\n"
    "ds_read_b128 a[208:211], v25 offset:24576\n"
    "buffer_load_dwordx4 v18, s[48:51], 0 offen offset:1024 lds\n"
    "v_mfma_f32_16x16x32_bf16 v[152:155], a[104:107], a[244:247], v[152:155]\n"
    "s_add_u32 m0, s57, 139264\n"
    "ds_read_b128 a[212:215], v26 offset:24576\n"
    "v_add_u32_dpp v19, v10, v2 row_newbcast:0 row_mask:0xf bank_mask:0xf\n"
    "v_mfma_f32_16x16x32_bf16 v[152:155], a[112:115], a[248:251], v[152:155]\n"
    "ds_read_b128 a[216:219], v27 offset:24576\n"
    "v_mfma_f32_16x16x32_bf16 v[152:155], a[120:123], a[252:255], v[152:155]\n"
    "ds_read_b128 a[220:223], v28 offset:24576\n"
    "v_mfma_f32_16x16x32_bf16 v[156:159], a[124:127], a[252:255], 0\n"
    "ds_read_b128 a[224:227], v25 offset:25600\n"
    "buffer_load_dwordx4 v19, s[48:51], 0 offen offset:0 lds\n"
    "v_mfma_f32_16x16x32_bf16 v[156:159], a[116:119], a[248:251], v[156:159]\n"
    "ds_read_b128 a[228:231], v26 offset:25600\n"
    "v_add_u32_dpp v20, v10, v3 row_newbcast:8 row_mask:0xf bank_mask:0xf\n"
    "v_mfma_f32_16x16x32_bf16 v[156:159], a[108:111], a[244:247], v[156:159]\n"
    "ds_read_b128 a[232:235], v27 offset:25600\n"
    "v_mfma_f32_16x16x32_bf16 v[156:159], a[100:103], a[240:243], v[156:159]\n"
    "ds_read_b128 a[236:239], v28 offset:25600\n"
    "v_mfma_f32_16x16x32_bf16 v[164:167], a[100:103], v[232:235], 0\n"
    "ds_read_b128 a[240:243], v25 offset:0\n"
    "v_mfma_f32_16x16x32_bf16 v[164:167], a[108:111], v[236:239], v[164:167]\n"
    "ds_read_b128 a[244:247], v26 offset:0\n"
    "buffer_load_dwordx4 v20, s[48:51], 0 offen offset:1024 lds\n"
    "v_mfma_f32_16x16x32_bf16 v[164:167], a[116:119], v[240:243], v[164:167]\n"
    "ds_read_b128 a[248:251], v27 offset:0\n"
    "v_mfma_f32_16x16x32_bf16 v[164:167], a[124:127], v[244:247], v[164:167]\n"
    "ds_read_b128 a[252:255], v28 offset:0\n"
    "v_mfma_f32_16x16x32_bf16 v[160:163], a[120:123], v[244:247], 0\n"
    "ds_read_b64_tr_b16 a[192:193], v47 offset:8192\n"
    "v_mfma_f32_16x16x32_bf16 v[160:163], a[112:115], v[240:243], v[160:163]\n"
    "ds_read_b64_tr_b16 a[194:195], v47 offset:8704\n"
    "buffer_load_dword v16, v7, s[40:43], s37 offen\n"
    "v_mfma_f32_16x16x32_bf16 v[160:163], a[104:107], v[236:239], v[160:163]\n"
    "ds_read_b64_tr_b16 a[196:197], v48 offset:8192\n"
    "v_mfma_f32_16x16x32_bf16 v[160:163], a[96:99], v[232:235], v[160:163]\n"
    "s_waitcnt lgkmcnt(6)\n"
    "ds_read_b128 v[232:235], v25 offset:1024\n"
    "v_mfma_f32_16x16x32_bf16 v[152:155], a[64:67], a[208:211], v[152:155]\n"
    "ds_read_b128 v[236:239], v26 offset:1024\n"
    "v_mfma_f32_16x16x32_bf16 v[152:155], a[72:75], a[212:215], v[152:155]\n"
    "ds_read_b128 v[240:243], v27 offset:1024\n"
    "buffer_load_dword v15, v6, s[40:43], s37 offen\n"
    "v_mfma_f32_16x16x32_bf16 v[152:155], a[80:83], a[216:219], v[152:155]\n"
    "s_add_u32 m0, s57, 155648\n"
    "ds_read_b128 v[244:247], v28 offset:1024\n"
    "v_add_u32_dpp v17, v9, v2 row_newbcast:4 row_mask:0xf bank_mask:0xf\n"
    "v_mfma_f32_16x16x32_bf16 v[152:155], a[88:91], a[220:223], v[152:155]\n"
    "ds_read_b64_tr_b16 a[198:199], v48 offset:8704\n"
    "v_mfma_f32_16x16x32_bf16 v[156:159], a[92:95], a[220:223], v[156:159]\n"
    "v_mfma_f32_16x16x32_bf16 v[156:159], a[84:87], a[216:219], v[156:159]\n"
    "v_mfma_f32_16x16x32_bf16 v[156:159], a[76:79], a[212:215], v[156:159]\n"
    "buffer_load_dwordx4 v17, s[48:51], 0 offen offset:0 lds\n"
    "v_add_u32_dpp v18, v9, v3 row_newbcast:12 row_mask:0xf bank_mask:0xf\n"
    "v_mfma_f32_16x16x32_bf16 v[156:159], a[68:71], a[208:211], v[156:159]\n"
    "v_mfma_f32_16x16x32_bf16 v[164:167], a[68:71], a[224:227], v[164:167]\n"
    "v_mfma_f32_16x16x32_bf16 v[164:167], a[76:79], a[228:231], v[164:167]\n"
    "v_mfma_f32_16x16x32_bf16 v[164:167], a[84:87], a[232:235], v[164:167]\n"
    "buffer_load_dwordx4 v18, s[48:51], 0 offen offset:1024 lds\n"
    "v_mfma_f32_16x16x32_bf16 v[164:167], a[92:95], a[236:239], v[164:167]\n"
    "v_mfma_f32_16x16x32_bf16 v[160:163], a[88:91], a[236:239], v[160:163]\n"
    "v_mfma_f32_16x16x32_bf16 v[160:163], a[80:83], a[232:235], v[160:163]\n"
    "v_mfma_f32_16x16x32_bf16 v[160:163], a[72:75], a[228:231], v[160:163]\n"
    "v_mfma_f32_16x16x32_bf16 v[160:163], a[64:67], a[224:227], v[160:163]\n"
    "s_add_u32 m0, s57, 0\n"
    "v_add_u32_dpp v19, v9, v2 row_newbcast:5 row_mask:0xf bank_mask:0xf\n"
    "buffer_load_dwordx4 v19, s[48:51], 0 offen offset:0 lds\n"
    "v_add_u32_dpp v20, v9, v3 row_newbcast:13 row_mask:0xf bank_mask:0xf\n"
    "buffer_load_dwordx4 v20, s[48:51], 0 offen offset:1024 lds\n"
    "s_waitcnt vmcnt(8) lgkmcnt(0)\n"
    "s_barrier\n"
    "v_mfma_f32_16x16x32_bf16 v[136:139], a[96:99], a[240:243], v[136:139]\n"
    "s_mov_b32 s100, 0\n"
    "ds_read_b128 a[208:211], v29 offset:16384\n"
    "s_mov_b32 s101, 0x4d000000\n"
    "v_mfma_f32_16x16x32_bf16 v[136:139], a[104:107], a[244:247], v[136:139]\n"
    "s_xor_b32 s34, %1, 1\n"
    "ds_read_b128 a[212:215], v30 offset:16384\n"
    "s_mul_i32 s34, s34, 49152\n"
    "v_mfma_f32_16x16x32_bf16 v[136:139], a[112:115], a[248:251], v[136:139]\n"
    "s_add_u32 m0, s57, 8192\n"
    "ds_read_b128 a[216:219], v31 offset:16384\n"
    "v_add_u32_dpp v17, v11, v2 row_newbcast:0 row_mask:0xf bank_mask:0xf\n"
    "v_mfma_f32_16x16x32_bf16 v[136:139], a[120:123], a[252:255], v[136:139]\n"
    "s_xor_b32 s35, %1, 2\n"
    "ds_read_b128 a[220:223], v32 offset:16384\n"
    "s_mul_i32 s35, s35, 49152\n"
    "v_mfma_f32_16x16x32_bf16 v[140:143], a[124:127], a[252:255], v[140:143]\n"
    "s_add_u32 s35, s35, 16384\n"
    "ds_read_b128 a[224:227], v29 offset:17408\n"
    "s_xor_b32 s36, %1, 3\n"
    "v_mfma_f32_16x16x32_bf16 v[140:143], a[116:119], a[248:251], v[140:143]\n"
    "s_mul_i32 s36, s36, 49152\n"
    "ds_read_b128 a[228:231], v30 offset:17408\n"
    "s_add_u32 s36, s36, 32768\n"
    "v_mfma_f32_16x16x32_bf16 v[140:143], a[108:111], a[244:247], v[140:143]\n"
    "ds_read_b128 a[232:235], v31 offset:17408\n"
    "buffer_load_dwordx4 v17, s[48:51], 0 offen offset:0 lds\n"
    "v_mfma_f32_16x16x32_bf16 v[140:143], a[100:103], a[240:243], v[140:143]\n"
    "ds_read_b128 a[236:239], v32 offset:17408\n"
    "v_add_u32_dpp v18, v11, v3 row_newbcast:8 row_mask:0xf bank_mask:0xf\n"
    "v_mfma_f32_16x16x32_bf16 v[148:151], a[100:103], v[232:235], v[148:151]\n"
    "ds_read_b128 a[240:243], v25 offset:16384\n"
    "v_mfma_f32_16x16x32_bf16 v[148:151], a[108:111], v[236:239], v[148:151]\n"
    "ds_read_b128 a[244:247], v26 offset:16384\n"
    "v_mfma_f32_16x16x32_bf16 v[148:151], a[116:119], v[240:243], v[148:151]\n"
    "ds_read_b128 a[248:251], v27 offset:16384\n"
    "v_mfma_f32_16x16x32_bf16 v[148:151], a[124:127], v[244:247], v[148:151]\n"
    "ds_read_b128 a[252:255], v28 offset:16384\n"
    "v_mfma_f32_16x16x32_bf16 v[144:147], a[120:123], v[244:247], v[144:147]\n"
    "ds_read_b64_tr_b16 a[200:201], v47 offset:32768\n"
    "v_mfma_f32_16x16x32_bf16 v[144:147], a[112:115], v[240:243], v[144:147]\n"
    "ds_read_b64_tr_b16 a[202:203], v47 offset:33280\n"
    "v_mfma_f32_16x16x32_bf16 v[144:147], a[104:107], v[236:239], v[144:147]\n"
    "ds_read_b64_tr_b16 a[204:205], v48 offset:32768\n"
    "v_mfma_f32_16x16x32_bf16 v[144:147], a[96:99], v[232:235], v[144:147]\n"
    "s_waitcnt lgkmcnt(6)\n"
    "ds_read_b128 v[232:235], v25 offset:17408\n"
    "buffer_load_dwordx4 v18, s[48:51], 0 offen offset:1024 lds\n"
    "v_mfma_f32_16x16x32_bf16 v[104:107], a[0:3], a[208:211], v[104:107]\n"
    "ds_read_b128 v[236:239], v26 offset:17408\n"
    "v_mfma_f32_16x16x32_bf16 v[104:107], a[8:11], a[212:215], v[104:107]\n"
    "ds_read_b128 v[240:243], v27 offset:17408\n"
    "v_mfma_f32_16x16x32_bf16 v[104:107], a[16:19], a[216:219], v[104:107]\n"
    "ds_read_b128 v[244:247], v28 offset:17408\n"
    "v_mfma_f32_16x16x32_bf16 v[104:107], a[24:27], a[220:223], v[104:107]\n"
    "ds_read_b64_tr_b16 a[206:207], v48 offset:33280\n"
    "v_mfma_f32_16x16x32_bf16 v[108:111], a[28:31], a[220:223], v[108:111]\n"
    "s_add_u32 m0, s57, 16384\n"
    "ds_read_b64_tr_b16 a[48:49], v47 offset:40960\n"
    "v_add_u32_dpp v19, v11, v2 row_newbcast:4 row_mask:0xf bank_mask:0xf\n"
    "v_mfma_f32_16x16x32_bf16 v[108:111], a[20:23], a[216:219], v[108:111]\n"
    "ds_read_b64_tr_b16 a[50:51], v47 offset:41472\n"
    "v_mfma_f32_16x16x32_bf16 v[108:111], a[12:15], a[212:215], v[108:111]\n"
    "ds_read_b64_tr_b16 a[52:53], v48 offset:40960\n"
    "v_mfma_f32_16x16x32_bf16 v[108:111], a[4:7], a[208:211], v[108:111]\n"
    "ds_read_b64_tr_b16 a[54:55], v48 offset:41472\n"
    "v_mfma_f32_16x16x32_bf16 v[116:119], a[4:7], a[224:227], v[116:119]\n"
    "buffer_load_dwordx4 v19, s[48:51], 0 offen offset:0 lds\n"
    "v_add_u32_dpp v20, v11, v3 row_newbcast:12 row_mask:0xf bank_mask:0xf\n"
    "v_mfma_f32_16x16x32_bf16 v[116:119], a[12:15], a[228:231], v[116:119]\n"
    "v_mfma_f32_16x16x32_bf16 v[116:119], a[20:23], a[232:235], v[116:119]\n"
    "v_mfma_f32_16x16x32_bf16 v[116:119], a[28:31], a[236:239], v[116:119]\n"
    "v_mfma_f32_16x16x32_bf16 v[112:115], a[24:27], a[236:239], v[112:115]\n"
    "v_mfma_f32_16x16x32_bf16 v[112:115], a[16:19], a[232:235], v[112:115]\n"
    "v_mfma_f32_16x16x32_bf16 v[112:115], a[8:11], a[228:231], v[112:115]\n"
    "v_mfma_f32_16x16x32_bf16 v[112:115], a[0:3], a[224:227], v[112:115]\n"
    "buffer_load_dwordx4 v20, s[48:51], 0 offen offset:1024 lds\n"
    "s_add_u32 m0, s57, 32768\n"
    "v_add_u32_dpp v17, v12, v2 row_newbcast:0 row_mask:0xf bank_mask:0xf\n"
    "buffer_load_dwordx4 v17, s[48:51], 0 offen offset:0 lds\n"
    "v_add_u32_dpp v18, v12, v3 row_newbcast:8 row_mask:0xf bank_mask:0xf\n"
    "buffer_load_dwordx4 v18, s[48:51], 0 offen offset:1024 lds\n"
    "s_waitcnt vmcnt(8) lgkmcnt(0)\n"
    "s_barrier\n"
    "v_mfma_f32_16x16x32_bf16 v[168:171], a[64:67], a[240:243], 0\n"
    "ds_read_b128 a[208:211], v29 offset:24576\n"
    "v_lshlrev_b32 v16, 8, v16\n"
    "v_mfma_f32_16x16x32_bf16 v[168:171], a[72:75], a[244:247], v[168:171]\n"
    "ds_read_b128 a[212:215], v30 offset:24576\n"
    "v_lshlrev_b32 v15, 8, v15\n"
    "v_mfma_f32_16x16x32_bf16 v[168:171], a[80:83], a[248:251], v[168:171]\n"
    "ds_read_b128 a[216:219], v31 offset:24576\n"
    "v_mfma_f32_16x16x32_bf16 v[168:171], a[88:91], a[252:255], v[168:171]\n"
    "ds_read_b128 a[220:223], v32 offset:24576\n"
    "v_mfma_f32_16x16x32_bf16 v[172:175], a[92:95], a[252:255], 0\n"
    "ds_read_b128 a[224:227], v29 offset:25600\n"
    "v_mfma_f32_16x16x32_bf16 v[172:175], a[84:87], a[248:251], v[172:175]\n"
    "ds_read_b128 a[228:231], v30 offset:25600\n"
    "v_mfma_f32_16x16x32_bf16 v[172:175], a[76:79], a[244:247], v[172:175]\n"
    "ds_read_b128 a[232:235], v31 offset:25600\n"
    "v_mfma_f32_16x16x32_bf16 v[172:175], a[68:71], a[240:243], v[172:175]\n"
    "ds_read_b128 a[236:239], v32 offset:25600\n"
    "v_mfma_f32_16x16x32_bf16 v[180:183], a[68:71], v[232:235], 0\n"
    "ds_read_b128 a[240:243], v25 offset:49152\n"
    "v_mfma_f32_16x16x32_bf16 v[180:183], a[76:79], v[236:239], v[180:183]\n"
    "ds_read_b128 a[244:247], v26 offset:49152\n"
    "v_mfma_f32_16x16x32_bf16 v[180:183], a[84:87], v[240:243], v[180:183]\n"
    "ds_read_b128 a[248:251], v27 offset:49152\n"
    "v_mfma_f32_16x16x32_bf16 v[180:183], a[92:95], v[244:247], v[180:183]\n"
    "ds_read_b128 a[252:255], v28 offset:49152\n"
    "v_mfma_f32_16x16x32_bf16 v[176:179], a[88:91], v[244:247], 0\n"
    "v_mfma_f32_16x16x32_bf16 v[176:179], a[80:83], v[240:243], v[176:179]\n"
    "v_mfma_f32_16x16x32_bf16 v[176:179], a[72:75], v[236:239], v[176:179]\n"
    "v_mfma_f32_16x16x32_bf16 v[176:179], a[64:67], v[232:235], v[176:179]\n"
    "s_waitcnt lgkmcnt(3)\n"
    "ds_read_b128 v[232:235], v25 offset:50176\n"
    "v_mfma_f32_16x16x32_bf16 v[104:107], a[128:131], a[208:211], v[104:107]\n"
    "s_add_u32 m0, s57, 40960\n"
    "ds_read_b128 v[236:239], v26 offset:50176\n"
    "v_add_u32_dpp v19, v9, v4 row_newbcast:6 row_mask:0x5 bank_mask:0x3\n"
    "v_mfma_f32_16x16x32_bf16 v[104:107], a[136:139], a[212:215], v[104:107]\n"
    "ds_read_b128 v[240:243], v27 offset:50176\n"
    "v_add_u32_dpp v19, v9, v4 row_newbcast:6 row_mask:0xa bank_mask:0xc\n"
    "v_mfma_f32_16x16x32_bf16 v[104:107], a[144:147], a[216:219], v[104:107]\n"
    "ds_read_b128 v[244:247], v28 offset:50176\n"
    "v_add_u32_dpp v19, v11, v4 row_newbcast:6 row_mask:0x5 bank_mask:0xc\n"
    "v_mfma_f32_16x16x32_bf16 v[104:107], a[152:155], a[220:223], v[104:107]\n"
    "v_add_u32_dpp v19, v11, v4 row_newbcast:6 row_mask:0xa bank_mask:0x3\n"
    "v_mfma_f32_16x16x32_bf16 v[108:111], a[156:159], a[220:223], v[108:111]\n"
    "v_mfma_f32_16x16x32_bf16 v[108:111], a[148:151], a[216:219], v[108:111]\n"
    "buffer_load_dwordx4 v19, s[48:51], 0 offen offset:0 lds\n"
    "v_add_u32_dpp v20, v13, v5 row_newbcast:6 row_mask:0x5 bank_mask:0x3\n"
    "v_mfma_f32_16x16x32_bf16 v[108:111], a[140:143], a[212:215], v[108:111]\n"
    "v_add_u32_dpp v20, v13, v5 row_newbcast:6 row_mask:0xa bank_mask:0xc\n"
    "v_add_u32_dpp v20, v15, v5 row_newbcast:6 row_mask:0x5 bank_mask:0xc\n"
    "v_mfma_f32_16x16x32_bf16 v[108:111], a[132:135], a[208:211], v[108:111]\n"
    "v_add_u32_dpp v20, v15, v5 row_newbcast:6 row_mask:0xa bank_mask:0x3\n"
    "v_mfma_f32_16x16x32_bf16 v[116:119], a[132:135], a[224:227], v[116:119]\n"
    "v_mfma_f32_16x16x32_bf16 v[116:119], a[140:143], a[228:231], v[116:119]\n"
    "v_mfma_f32_16x16x32_bf16 v[116:119], a[148:151], a[232:235], v[116:119]\n"
    "v_mfma_f32_16x16x32_bf16 v[116:119], a[156:159], a[236:239], v[116:119]\n"
    "v_mfma_f32_16x16x32_bf16 v[112:115], a[152:155], a[236:239], v[112:115]\n"
    "v_mfma_f32_16x16x32_bf16 v[112:115], a[144:147], a[232:235], v[112:115]\n"
    "v_mfma_f32_16x16x32_bf16 v[112:115], a[136:139], a[228:231], v[112:115]\n"
    "buffer_load_dwordx4 v20, s[48:51], 0 offen offset:1024 lds\n"
    "v_mfma_f32_16x16x32_bf16 v[112:115], a[128:131], a[224:227], v[112:115]\n"
    "s_waitcnt lgkmcnt(0)\n"
    "s_barrier\n"
    "v_mfma_f32_16x16x32_bf16 v[184:187], a[96:99], a[240:243], 0\n"
    "ds_read_b128 a[208:211], v25 offset:57344\n"
    "v_mfma_f32_16x16x32_bf16 v[184:187], a[104:107], a[244:247], v[184:187]\n"
    "s_add_u32 m0, s57, 24576\n"
    "ds_read_b128 a[212:215], v26 offset:57344\n"
    "v_add_u32_dpp v17, v9, v4 row_newbcast:14 row_mask:0x5 bank_mask:0x3\n"
    "v_mfma_f32_16x16x32_bf16 v[184:187], a[112:115], a[248:251], v[184:187]\n"
    "ds_read_b128 a[216:219], v27 offset:57344\n"
    "v_add_u32_dpp v17, v9, v4 row_newbcast:14 row_mask:0xa bank_mask:0xc\n"
    "v_mfma_f32_16x16x32_bf16 v[184:187], a[120:123], a[252:255], v[184:187]\n"
    "ds_read_b128 a[220:223], v28 offset:57344\n"
    "v_add_u32_dpp v17, v11, v4 row_newbcast:14 row_mask:0x5 bank_mask:0xc\n"
    "v_mfma_f32_16x16x32_bf16 v[188:191], a[124:127], a[252:255], 0\n"
    "ds_read_b128 a[224:227], v25 offset:58368\n"
    "v_add_u32_dpp v17, v11, v4 row_newbcast:14 row_mask:0xa bank_mask:0x3\n"
    "v_mfma_f32_16x16x32_bf16 v[188:191], a[116:119], a[248:251], v[188:191]\n"
    "ds_read_b128 a[228:231], v26 offset:58368\n"
    "buffer_load_dwordx4 v17, s[48:51], 0 offen offset:0 lds\n"
    "v_mfma_f32_16x16x32_bf16 v[188:191], a[108:111], a[244:247], v[188:191]\n"
    "ds_read_b128 a[232:235], v27 offset:58368\n"
    "v_add_u32_dpp v18, v13, v5 row_newbcast:14 row_mask:0x5 bank_mask:0x3\n"
    "v_mfma_f32_16x16x32_bf16 v[188:191], a[100:103], a[240:243], v[188:191]\n"
    "ds_read_b128 a[236:239], v28 offset:58368\n"
    "v_add_u32_dpp v18, v13, v5 row_newbcast:14 row_mask:0xa bank_mask:0xc\n"
    "v_mfma_f32_16x16x32_bf16 v[196:199], a[100:103], v[232:235], 0\n"
    "ds_read_b128 a[240:243], v29 offset:0\n"
    "v_add_u32_dpp v18, v15, v5 row_newbcast:14 row_mask:0x5 bank_mask:0xc\n"
    "v_mfma_f32_16x16x32_bf16 v[196:199], a[108:111], v[236:239], v[196:199]\n"
    "ds_read_b128 a[244:247], v30 offset:0\n"
    "v_add_u32_dpp v18, v15, v5 row_newbcast:14 row_mask:0xa bank_mask:0x3\n"
    "v_mfma_f32_16x16x32_bf16 v[196:199], a[116:119], v[240:243], v[196:199]\n"
    "ds_read_b128 a[248:251], v31 offset:0\n"
    "v_mfma_f32_16x16x32_bf16 v[196:199], a[124:127], v[244:247], v[196:199]\n"
    "ds_read_b128 a[252:255], v32 offset:0\n"
    "v_mfma_f32_16x16x32_bf16 v[192:195], a[120:123], v[244:247], 0\n"
    "v_mfma_f32_16x16x32_bf16 v[192:195], a[112:115], v[240:243], v[192:195]\n"
    "v_mfma_f32_16x16x32_bf16 v[192:195], a[104:107], v[236:239], v[192:195]\n"
    "buffer_load_dwordx4 v18, s[48:51], 0 offen offset:1024 lds\n"
    "v_mfma_f32_16x16x32_bf16 v[192:195], a[96:99], v[232:235], v[192:195]\n"
    "s_waitcnt lgkmcnt(3)\n"
    "ds_read_b128 v[232:235], v29 offset:1024\n"
    "v_mfma_f32_16x16x32_bf16 v[184:187], a[64:67], a[208:211], v[184:187]\n"
    "ds_read_b128 v[236:239], v30 offset:1024\n"
    "v_mfma_f32_16x16x32_bf16 v[184:187], a[72:75], a[212:215], v[184:187]\n"
    "ds_read_b128 v[240:243], v31 offset:1024\n"
    "v_mfma_f32_16x16x32_bf16 v[184:187], a[80:83], a[216:219], v[184:187]\n"
    "ds_read_b128 v[244:247], v32 offset:1024\n"
    "v_mfma_f32_16x16x32_bf16 v[184:187], a[88:91], a[220:223], v[184:187]\n"
    "v_mfma_f32_16x16x32_bf16 v[188:191], a[92:95], a[220:223], v[188:191]\n"
    "v_mfma_f32_16x16x32_bf16 v[188:191], a[84:87], a[216:219], v[188:191]\n"
    "v_mfma_f32_16x16x32_bf16 v[188:191], a[76:79], a[212:215], v[188:191]\n"
    "v_mfma_f32_16x16x32_bf16 v[188:191], a[68:71], a[208:211], v[188:191]\n"
    "global_load_dwordx2 v[76:77], v65, s[20:21] offset:0\n"
    "v_mfma_f32_16x16x32_bf16 v[196:199], a[68:71], a[224:227], v[196:199]\n"
    "global_load_dwordx2 v[78:79], v65, s[20:21] offset:32\n"
    "v_mfma_f32_16x16x32_bf16 v[196:199], a[76:79], a[228:231], v[196:199]\n"
    "global_load_dwordx2 v[80:81], v65, s[22:23] offset:0\n"
    "v_mfma_f32_16x16x32_bf16 v[196:199], a[84:87], a[232:235], v[196:199]\n"
    "global_load_dwordx2 v[82:83], v65, s[22:23] offset:32\n"
    "v_mfma_f32_16x16x32_bf16 v[196:199], a[92:95], a[236:239], v[196:199]\n"
    "global_load_ushort v62, v64, s[24:25]\n"
    "v_mfma_f32_16x16x32_bf16 v[192:195], a[88:91], a[236:239], v[192:195]\n"
    "v_mfma_f32_16x16x32_bf16 v[192:195], a[80:83], a[232:235], v[192:195]\n"
    "v_mfma_f32_16x16x32_bf16 v[192:195], a[72:75], a[228:231], v[192:195]\n"
    "v_mfma_f32_16x16x32_bf16 v[192:195], a[64:67], a[224:227], v[192:195]\n"
    "s_waitcnt vmcnt(15) lgkmcnt(0)\n"
    "s_barrier\n"
    "v_mfma_f32_16x16x32_bf16 v[168:171], a[96:99], a[240:243], v[168:171]\n"
    "ds_read_b128 a[208:211], v29 offset:8192\n"
    "global_load_ushort v63, v58, s[26:27]\n"
    "v_mfma_f32_16x16x32_bf16 v[168:171], a[104:107], a[244:247], v[168:171]\n"
    "ds_read_b128 a[212:215], v30 offset:8192\n"
    "v_mfma_f32_16x16x32_bf16 v[168:171], a[112:115], a[248:251], v[168:171]\n"
    "s_add_u32 m0, s57, 49152\n"
    "ds_read_b128 a[216:219], v31 offset:8192\n"
    "v_add_u32_dpp v19, v11, v2 row_newbcast:5 row_mask:0xf bank_mask:0xf\n"
    "v_mfma_f32_16x16x32_bf16 v[168:171], a[120:123], a[252:255], v[168:171]\n"
    "ds_read_b128 a[220:223], v32 offset:8192\n"
    "v_mfma_f32_16x16x32_bf16 v[172:175], a[124:127], a[252:255], v[172:175]\n"
    "ds_read_b128 a[224:227], v29 offset:9216\n"
    "v_mfma_f32_16x16x32_bf16 v[172:175], a[116:119], a[248:251], v[172:175]\n"
    "ds_read_b128 a[228:231], v30 offset:9216\n"
    "v_mfma_f32_16x16x32_bf16 v[172:175], a[108:111], a[244:247], v[172:175]\n"
    "ds_read_b128 a[232:235], v31 offset:9216\n"
    "v_mfma_f32_16x16x32_bf16 v[172:175], a[100:103], a[240:243], v[172:175]\n"
    "ds_read_b128 a[236:239], v32 offset:9216\n"
    "buffer_load_dwordx4 v19, s[48:51], 0 offen offset:0 lds\n"
    "v_mfma_f32_16x16x32_bf16 v[180:183], a[100:103], v[232:235], v[180:183]\n"
    "ds_read_b128 a[240:243], v21 offset:0\n"
    "v_add_u32_dpp v20, v11, v3 row_newbcast:13 row_mask:0xf bank_mask:0xf\n"
    "v_mfma_f32_16x16x32_bf16 v[180:183], a[108:111], v[236:239], v[180:183]\n"
    "ds_read_b128 a[244:247], v22 offset:0\n"
    "v_mfma_f32_16x16x32_bf16 v[180:183], a[116:119], v[240:243], v[180:183]\n"
    "ds_read_b128 a[248:251], v23 offset:0\n"
    "v_mfma_f32_16x16x32_bf16 v[180:183], a[124:127], v[244:247], v[180:183]\n"
    "ds_read_b128 a[252:255], v24 offset:0\n"
    "v_mfma_f32_16x16x32_bf16 v[176:179], a[120:123], v[244:247], v[176:179]\n"
    "v_mfma_f32_16x16x32_bf16 v[176:179], a[112:115], v[240:243], v[176:179]\n"
    "v_mfma_f32_16x16x32_bf16 v[176:179], a[104:107], v[236:239], v[176:179]\n"
    "v_mfma_f32_16x16x32_bf16 v[176:179], a[96:99], v[232:235], v[176:179]\n"
    "s_waitcnt lgkmcnt(3)\n"
    "ds_read_b128 v[232:235], v21 offset:1024\n"
    "v_mfma_f32_16x16x32_bf16 v[120:123], a[0:3], a[208:211], v[120:123]\n"
    "ds_read_b128 v[236:239], v22 offset:1024\n"
    "buffer_load_dwordx4 v20, s[48:51], 0 offen offset:1024 lds\n"
    "v_mfma_f32_16x16x32_bf16 v[120:123], a[8:11], a[212:215], v[120:123]\n"
    "ds_read_b128 v[240:243], v23 offset:1024\n"
    "v_and_b32 v55, 28, v55\n"
    "v_mfma_f32_16x16x32_bf16 v[120:123], a[16:19], a[216:219], v[120:123]\n"
    "ds_read_b128 v[244:247], v24 offset:1024\n"
    "buffer_load_dword v56, v55, s[52:55], s39 offen offset:128 sc1\n"
    "v_mfma_f32_16x16x32_bf16 v[120:123], a[24:27], a[220:223], v[120:123]\n"
    "v_mfma_f32_16x16x32_bf16 v[124:127], a[28:31], a[220:223], v[124:127]\n"
    "v_mfma_f32_16x16x32_bf16 v[124:127], a[20:23], a[216:219], v[124:127]\n"
    "v_mfma_f32_16x16x32_bf16 v[124:127], a[12:15], a[212:215], v[124:127]\n"
    "v_mfma_f32_16x16x32_bf16 v[124:127], a[4:7], a[208:211], v[124:127]\n"
    "v_mfma_f32_16x16x32_bf16 v[132:135], a[4:7], a[224:227], v[132:135]\n"
    "v_mfma_f32_16x16x32_bf16 v[132:135], a[12:15], a[228:231], v[132:135]\n"
    "v_mfma_f32_16x16x32_bf16 v[132:135], a[20:23], a[232:235], v[132:135]\n"
    "v_mfma_f32_16x16x32_bf16 v[132:135], a[28:31], a[236:239], v[132:135]\n"
    "v_mfma_f32_16x16x32_bf16 v[128:131], a[24:27], a[236:239], v[128:131]\n"
    "v_mfma_f32_16x16x32_bf16 v[128:131], a[16:19], a[232:235], v[128:131]\n"
    "v_mfma_f32_16x16x32_bf16 v[128:131], a[8:11], a[228:231], v[128:131]\n"
    "v_mfma_f32_16x16x32_bf16 v[128:131], a[0:3], a[224:227], v[128:131]\n"
    "s_add_u32 m0, s57, 57344\n"
    "v_add_u32_dpp v17, v13, v2 row_newbcast:0 row_mask:0xf bank_mask:0xf\n"
    "buffer_load_dwordx4 v17, s[48:51], 0 offen offset:0 lds\n"
    "v_add_u32_dpp v18, v13, v3 row_newbcast:8 row_mask:0xf bank_mask:0xf\n"
    "buffer_load_dwordx4 v18, s[48:51], 0 offen offset:1024 lds\n"
    "s_add_u32 m0, s57, 90112\n"
    "v_add_u32_dpp v19, v14, v2 row_newbcast:0 row_mask:0xf bank_mask:0xf\n"
    "buffer_load_dwordx4 v19, s[48:51], 0 offen offset:0 lds\n"
    "v_add_u32_dpp v20, v14, v3 row_newbcast:8 row_mask:0xf bank_mask:0xf\n"
    "buffer_load_dwordx4 v20, s[48:51], 0 offen offset:1024 lds\n"
    "s_waitcnt vmcnt(13) lgkmcnt(0)\n"
    "s_barrier\n"
    "v_mfma_f32_16x16x32_bf16 v[104:107], a[160:163], a[240:243], v[104:107]\n"
    "ds_read_b128 a[208:211], v33 offset:40960\n"
    "v_mfma_f32_16x16x32_bf16 v[104:107], a[168:171], a[244:247], v[104:107]\n"
    "ds_read_b128 a[212:215], v34 offset:40960\n"
    "v_mfma_f32_16x16x32_bf16 v[104:107], a[176:179], a[248:251], v[104:107]\n"
    "ds_read_b128 a[216:219], v35 offset:40960\n"
    "v_mfma_f32_16x16x32_bf16 v[104:107], a[184:187], a[252:255], v[104:107]\n"
    "ds_read_b128 a[220:223], v36 offset:40960\n"
    "v_mfma_f32_16x16x32_bf16 v[108:111], a[188:191], a[252:255], v[108:111]\n"
    "s_add_u32 m0, s57, 65536\n"
    "ds_read_b128 a[224:227], v33 offset:41984\n"
    "v_add_u32_dpp v17, v13, v2 row_newbcast:5 row_mask:0xf bank_mask:0xf\n"
    "v_mfma_f32_16x16x32_bf16 v[108:111], a[180:183], a[248:251], v[108:111]\n"
    "ds_read_b128 a[228:231], v34 offset:41984\n"
    "v_mfma_f32_16x16x32_bf16 v[108:111], a[172:175], a[244:247], v[108:111]\n"
    "ds_read_b128 a[232:235], v35 offset:41984\n"
    "v_mfma_f32_16x16x32_bf16 v[108:111], a[164:167], a[240:243], v[108:111]\n"
    "ds_read_b128 a[236:239], v36 offset:41984\n"
    "v_mfma_f32_16x16x32_bf16 v[116:119], a[164:167], v[232:235], v[116:119]\n"
    "ds_read_b128 a[240:243], v33 offset:24576\n"
    "buffer_load_dwordx4 v17, s[48:51], 0 offen offset:0 lds\n"
    "v_mfma_f32_16x16x32_bf16 v[116:119], a[172:175], v[236:239], v[116:119]\n"
    "ds_read_b128 a[244:247], v34 offset:24576\n"
    "v_add_u32_dpp v18, v13, v3 row_newbcast:13 row_mask:0xf bank_mask:0xf\n"
    "v_mfma_f32_16x16x32_bf16 v[116:119], a[180:183], v[240:243], v[116:119]\n"
    "ds_read_b128 a[248:251], v35 offset:24576\n"
    "v_mfma_f32_16x16x32_bf16 v[116:119], a[188:191], v[244:247], v[116:119]\n"
    "ds_read_b128 a[252:255], v36 offset:24576\n"
    "v_mfma_f32_16x16x32_bf16 v[112:115], a[184:187], v[244:247], v[112:115]\n"
    "v_mfma_f32_16x16x32_bf16 v[112:115], a[176:179], v[240:243], v[112:115]\n"
    "v_mfma_f32_16x16x32_bf16 v[112:115], a[168:171], v[236:239], v[112:115]\n"
    "v_mfma_f32_16x16x32_bf16 v[112:115], a[160:163], v[232:235], v[112:115]\n"
    "s_waitcnt lgkmcnt(3)\n"
    "ds_read_b128 v[232:235], v33 offset:25600\n"
    "v_mfma_f32_16x16x32_bf16 v[104:107], a[192:195], a[208:211], v[104:107]\n"
    "ds_read_b128 v[236:239], v34 offset:25600\n"
    "v_mfma_f32_16x16x32_bf16 v[104:107], a[200:203], a[212:215], v[104:107]\n"
    "ds_read_b128 v[240:243], v35 offset:25600\n"
    "v_mfma_f32_16x16x32_bf16 v[108:111], a[204:207], a[212:215], v[108:111]\n"
    "ds_read_b128 v[244:247], v36 offset:25600\n"
    "v_mfma_f32_16x16x32_bf16 v[108:111], a[196:199], a[208:211], v[108:111]\n"
    "v_mfma_f32_16x16x32_bf16 v[140:143], a[196:199], a[216:219], v[140:143]\n"
    "v_mfma_f32_16x16x32_bf16 v[140:143], a[204:207], a[220:223], v[140:143]\n"
    "v_mfma_f32_16x16x32_bf16 v[136:139], a[200:203], a[220:223], v[136:139]\n"
    "v_mfma_f32_16x16x32_bf16 v[136:139], a[192:195], a[216:219], v[136:139]\n"
    "v_mfma_f32_16x16x32_bf16 v[168:171], a[192:195], a[224:227], v[168:171]\n"
    "v_mfma_f32_16x16x32_bf16 v[168:171], a[200:203], a[228:231], v[168:171]\n"
    "v_mfma_f32_16x16x32_bf16 v[172:175], a[204:207], a[228:231], v[172:175]\n"
    "buffer_load_dwordx4 v18, s[48:51], 0 offen offset:1024 lds\n"
    "v_mfma_f32_16x16x32_bf16 v[172:175], a[196:199], a[224:227], v[172:175]\n"
    "v_mfma_f32_16x16x32_bf16 v[204:207], a[196:199], a[232:235], 0\n"
    "v_mfma_f32_16x16x32_bf16 v[204:207], a[204:207], a[236:239], v[204:207]\n"
    "v_mfma_f32_16x16x32_bf16 v[200:203], a[200:203], a[236:239], 0\n"
    "v_mfma_f32_16x16x32_bf16 v[200:203], a[192:195], a[232:235], v[200:203]\n"
    "s_waitcnt lgkmcnt(0)\n"
    "s_barrier\n"
    "v_mfma_f32_16x16x32_bf16 v[112:115], a[192:195], a[240:243], v[112:115]\n"
    "s_add_u32 m0, s57, 73728\n"
    "ds_read_b128 a[208:211], v21 offset:8192\n"
    "v_add_u32_dpp v19, v13, v2 row_newbcast:4 row_mask:0xf bank_mask:0xf\n"
    "v_mfma_f32_16x16x32_bf16 v[112:115], a[200:203], a[244:247], v[112:115]\n"
    "ds_read_b128 a[212:215], v22 offset:8192\n"
    "buffer_load_dwordx4 v19, s[48:51], 0 offen offset:0 lds\n"
    "v_mfma_f32_16x16x32_bf16 v[116:119], a[204:207], a[244:247], v[116:119]\n"
    "ds_read_b128 a[216:219], v23 offset:8192\n"
    "v_add_u32_dpp v20, v13, v3 row_newbcast:12 row_mask:0xf bank_mask:0xf\n"
    "v_mfma_f32_16x16x32_bf16 v[116:119], a[196:199], a[240:243], v[116:119]\n"
    "ds_read_b128 a[220:223], v24 offset:8192\n"
    "buffer_load_dwordx4 v20, s[48:51], 0 offen offset:1024 lds\n"
    "v_mfma_f32_16x16x32_bf16 v[148:151], a[196:199], a[248:251], v[148:151]\n"
    "ds_read_b128 a[224:227], v21 offset:9216\n"
    "v_mfma_f32_16x16x32_bf16 v[148:151], a[204:207], a[252:255], v[148:151]\n"
    "ds_read_b128 a[228:231], v22 offset:9216\n"
    "v_mfma_f32_16x16x32_bf16 v[144:147], a[200:203], a[252:255], v[144:147]\n"
    "ds_read_b128 a[232:235], v23 offset:9216\n"
    "v_mfma_f32_16x16x32_bf16 v[144:147], a[192:195], a[248:251], v[144:147]\n"
    "ds_read_b128 a[236:239], v24 offset:9216\n"
    "v_mfma_f32_16x16x32_bf16 v[176:179], a[192:195], v[232:235], v[176:179]\n"
    "ds_read_b128 a[240:243], v21 offset:16384\n"
    "v_mfma_f32_16x16x32_bf16 v[176:179], a[200:203], v[236:239], v[176:179]\n"
    "ds_read_b128 a[244:247], v22 offset:16384\n"
    "v_mfma_f32_16x16x32_bf16 v[180:183], a[204:207], v[236:239], v[180:183]\n"
    "ds_read_b128 a[248:251], v23 offset:16384\n"
    "v_mfma_f32_16x16x32_bf16 v[180:183], a[196:199], v[232:235], v[180:183]\n"
    "ds_read_b128 a[252:255], v24 offset:16384\n"
    "v_mfma_f32_16x16x32_bf16 v[212:215], a[196:199], v[240:243], 0\n"
    "v_mfma_f32_16x16x32_bf16 v[212:215], a[204:207], v[244:247], v[212:215]\n"
    "v_mfma_f32_16x16x32_bf16 v[208:211], a[200:203], v[244:247], 0\n"
    "v_mfma_f32_16x16x32_bf16 v[208:211], a[192:195], v[240:243], v[208:211]\n"
    "s_waitcnt lgkmcnt(3)\n"
    "ds_read_b128 v[232:235], v21 offset:17408\n"
    "v_mfma_f32_16x16x32_bf16 v[136:139], a[0:3], a[208:211], v[136:139]\n"
    "ds_read_b128 v[236:239], v22 offset:17408\n"
    "v_mfma_f32_16x16x32_bf16 v[136:139], a[8:11], a[212:215], v[136:139]\n"
    "ds_read_b128 v[240:243], v23 offset:17408\n"
    "v_mfma_f32_16x16x32_bf16 v[136:139], a[16:19], a[216:219], v[136:139]\n"
    "s_add_u32 m0, s57, 147456\n"
    "ds_read_b128 v[244:247], v24 offset:17408\n"
    "s_xor_b32 s96, s96, 32768\n"
    "v_mfma_f32_16x16x32_bf16 v[136:139], a[24:27], a[220:223], v[136:139]\n"
    "v_mfma_f32_16x16x32_bf16 v[140:143], a[28:31], a[220:223], v[140:143]\n"
    "v_mfma_f32_16x16x32_bf16 v[140:143], a[20:23], a[216:219], v[140:143]\n"
    "v_mfma_f32_16x16x32_bf16 v[140:143], a[12:15], a[212:215], v[140:143]\n"
    "buffer_load_dwordx4 v51, s[44:47], s96 offen offset:0 lds\n"
    "v_mfma_f32_16x16x32_bf16 v[140:143], a[4:7], a[208:211], v[140:143]\n"
    "v_mfma_f32_16x16x32_bf16 v[148:151], a[4:7], a[224:227], v[148:151]\n"
    "v_mfma_f32_16x16x32_bf16 v[148:151], a[12:15], a[228:231], v[148:151]\n"
    "v_mfma_f32_16x16x32_bf16 v[148:151], a[20:23], a[232:235], v[148:151]\n"
    "v_mfma_f32_16x16x32_bf16 v[148:151], a[28:31], a[236:239], v[148:151]\n"
    "v_mfma_f32_16x16x32_bf16 v[144:147], a[24:27], a[236:239], v[144:147]\n"
    "v_mfma_f32_16x16x32_bf16 v[144:147], a[16:19], a[232:235], v[144:147]\n"
    "v_mfma_f32_16x16x32_bf16 v[144:147], a[8:11], a[228:231], v[144:147]\n"
    "v_mfma_f32_16x16x32_bf16 v[144:147], a[0:3], a[224:227], v[144:147]\n"
    "buffer_load_dwordx4 v52, s[44:47], s96 offen offset:1024 lds\n"
    "s_waitcnt vmcnt(11) lgkmcnt(0)\n"
    "s_barrier\n"
    "v_mfma_f32_16x16x32_bf16 v[136:139], a[128:131], a[240:243], v[136:139]\n"
    "ds_read_b128 a[208:211], v21 offset:49152\n"
    "v_lshlrev_b32 v84, 16, v76\n"
    "v_mfma_f32_16x16x32_bf16 v[136:139], a[136:139], a[244:247], v[136:139]\n"
    "s_add_u32 m0, s57, 81920\n"
    "ds_read_b128 a[212:215], v22 offset:49152\n"
    "v_add_u32_dpp v17, v9, v2 row_newbcast:1 row_mask:0xf bank_mask:0xf\n"
    "v_mfma_f32_16x16x32_bf16 v[136:139], a[144:147], a[248:251], v[136:139]\n"
    "ds_read_b128 a[216:219], v23 offset:49152\n"
    "v_and_b32 v85, 0xffff0000, v76\n"
    "v_mfma_f32_16x16x32_bf16 v[136:139], a[152:155], a[252:255], v[136:139]\n"
    "ds_read_b128 a[220:223], v24 offset:49152\n"
    "v_lshlrev_b32 v86, 16, v77\n"
    "v_mfma_f32_16x16x32_bf16 v[140:143], a[156:159], a[252:255], v[140:143]\n"
    "ds_read_b128 a[224:227], v21 offset:50176\n"
    "v_and_b32 v87, 0xffff0000, v77\n"
    "v_mfma_f32_16x16x32_bf16 v[140:143], a[148:151], a[248:251], v[140:143]\n"
    "ds_read_b128 a[228:231], v22 offset:50176\n"
    "v_lshlrev_b32 v88, 16, v78\n"
    "v_mfma_f32_16x16x32_bf16 v[140:143], a[140:143], a[244:247], v[140:143]\n"
    "ds_read_b128 a[232:235], v23 offset:50176\n"
    "buffer_load_dwordx4 v17, s[48:51], 0 offen offset:0 lds\n"
    "v_mfma_f32_16x16x32_bf16 v[140:143], a[132:135], a[240:243], v[140:143]\n"
    "ds_read_b128 a[236:239], v24 offset:50176\n"
    "v_add_u32_dpp v18, v9, v3 row_newbcast:9 row_mask:0xf bank_mask:0xf\n"
    "v_mfma_f32_16x16x32_bf16 v[148:151], a[132:135], v[232:235], v[148:151]\n"
    "ds_read_b128 a[240:243], v21 offset:32768\n"
    "v_and_b32 v89, 0xffff0000, v78\n"
    "v_mfma_f32_16x16x32_bf16 v[148:151], a[140:143], v[236:239], v[148:151]\n"
    "ds_read_b128 a[244:247], v22 offset:32768\n"
    "v_lshlrev_b32 v90, 16, v79\n"
    "v_mfma_f32_16x16x32_bf16 v[148:151], a[148:151], v[240:243], v[148:151]\n"
    "ds_read_b128 a[248:251], v23 offset:32768\n"
    "v_and_b32 v91, 0xffff0000, v79\n"
    "v_mfma_f32_16x16x32_bf16 v[148:151], a[156:159], v[244:247], v[148:151]\n"
    "ds_read_b128 a[252:255], v24 offset:32768\n"
    "v_lshlrev_b32 v62, 16, v62\n"
    "v_mfma_f32_16x16x32_bf16 v[144:147], a[152:155], v[244:247], v[144:147]\n"
    "v_lshlrev_b32 v63, 16, v63\n"
    "v_and_b32 v62, s16, v62\n"
    "v_mfma_f32_16x16x32_bf16 v[144:147], a[144:147], v[240:243], v[144:147]\n"
    "v_and_b32 v63, s60, v63\n"
    "v_or_b32 v62, s17, v62\n"
    "v_mfma_f32_16x16x32_bf16 v[144:147], a[136:139], v[236:239], v[144:147]\n"
    "v_mul_f32 v100, v62, v63\n"
    "v_mfma_f32_16x16x32_bf16 v[144:147], a[128:131], v[232:235], v[144:147]\n"
    "s_waitcnt lgkmcnt(3)\n"
    "ds_read_b128 v[232:235], v21 offset:33792\n"
    "buffer_load_dwordx4 v18, s[48:51], 0 offen offset:1024 lds\n"
    "v_mfma_f32_16x16x32_bf16 v[136:139], a[160:163], a[208:211], v[136:139]\n"
    "ds_read_b128 v[236:239], v22 offset:33792\n"
    "v_mfma_f32_16x16x32_bf16 v[136:139], a[168:171], a[212:215], v[136:139]\n"
    "ds_read_b128 v[240:243], v23 offset:33792\n"
    "v_mfma_f32_16x16x32_bf16 v[136:139], a[176:179], a[216:219], v[136:139]\n"
    "ds_read_b128 v[244:247], v24 offset:33792\n"
    "v_mfma_f32_16x16x32_bf16 v[136:139], a[184:187], a[220:223], v[136:139]\n"
    "v_mfma_f32_16x16x32_bf16 v[140:143], a[188:191], a[220:223], v[140:143]\n"
    "v_mfma_f32_16x16x32_bf16 v[140:143], a[180:183], a[216:219], v[140:143]\n"
    "v_mfma_f32_16x16x32_bf16 v[140:143], a[172:175], a[212:215], v[140:143]\n"
    "s_add_u32 m0, s57, 98304\n"
    "v_add_u32_dpp v19, v10, v2 row_newbcast:4 row_mask:0xf bank_mask:0xf\n"
    "v_mfma_f32_16x16x32_bf16 v[140:143], a[164:167], a[208:211], v[140:143]\n"
    "v_mfma_f32_16x16x32_bf16 v[148:151], a[164:167], a[224:227], v[148:151]\n"
    "v_mfma_f32_16x16x32_bf16 v[148:151], a[172:175], a[228:231], v[148:151]\n"
    "v_mfma_f32_16x16x32_bf16 v[148:151], a[180:183], a[232:235], v[148:151]\n"
    "buffer_load_dwordx4 v19, s[48:51], 0 offen offset:0 lds\n"
    "v_add_u32_dpp v20, v10, v3 row_newbcast:12 row_mask:0xf bank_mask:0xf\n"
    "v_mfma_f32_16x16x32_bf16 v[148:151], a[188:191], a[236:239], v[148:151]\n"
    "v_mfma_f32_16x16x32_bf16 v[144:147], a[184:187], a[236:239], v[144:147]\n"
    "v_mfma_f32_16x16x32_bf16 v[144:147], a[176:179], a[232:235], v[144:147]\n"
    "v_mfma_f32_16x16x32_bf16 v[144:147], a[168:171], a[228:231], v[144:147]\n"
    "v_mfma_f32_16x16x32_bf16 v[144:147], a[160:163], a[224:227], v[144:147]\n"
    "s_waitcnt vmcnt(9) lgkmcnt(0)\n"
    "s_barrier\n"
    "v_mfma_f32_16x16x32_bf16 v[152:155], a[0:3], a[240:243], v[152:155]\n"
    "ds_read_b128 a[208:211], v21 offset:57344\n"
    "v_mfma_f32_16x16x32_bf16 v[152:155], a[8:11], a[244:247], v[152:155]\n"
    "ds_read_b128 a[212:215], v22 offset:57344\n"
    "v_mfma_f32_16x16x32_bf16 v[152:155], a[16:19], a[248:251], v[152:155]\n"
    "ds_read_b128 a[216:219], v23 offset:57344\n"
    "v_mfma_f32_16x16x32_bf16 v[152:155], a[24:27], a[252:255], v[152:155]\n"
    "ds_read_b128 a[220:223], v24 offset:57344\n"
    "v_mfma_f32_16x16x32_bf16 v[156:159], a[28:31], a[252:255], v[156:159]\n"
    "ds_read_b128 a[224:227], v21 offset:58368\n"
    "v_mfma_f32_16x16x32_bf16 v[156:159], a[20:23], a[248:251], v[156:159]\n"
    "ds_read_b128 a[228:231], v22 offset:58368\n"
    "v_mfma_f32_16x16x32_bf16 v[156:159], a[12:15], a[244:247], v[156:159]\n"
    "ds_read_b128 a[232:235], v23 offset:58368\n"
    "v_mfma_f32_16x16x32_bf16 v[156:159], a[4:7], a[240:243], v[156:159]\n"
    "ds_read_b128 a[236:239], v24 offset:58368\n"
    "v_mfma_f32_16x16x32_bf16 v[164:167], a[4:7], v[232:235], v[164:167]\n"
    "ds_read_b128 a[240:243], v25 offset:24576\n"
    "v_mfma_f32_16x16x32_bf16 v[164:167], a[12:15], v[236:239], v[164:167]\n"
    "ds_read_b128 a[244:247], v26 offset:24576\n"
    "v_mfma_f32_16x16x32_bf16 v[164:167], a[20:23], v[240:243], v[164:167]\n"
    "ds_read_b128 a[248:251], v27 offset:24576\n"
    "buffer_load_dwordx4 v20, s[48:51], 0 offen offset:1024 lds\n"
    "v_mfma_f32_16x16x32_bf16 v[164:167], a[28:31], v[244:247], v[164:167]\n"
    "ds_read_b128 a[252:255], v28 offset:24576\n"
    "v_mfma_f32_16x16x32_bf16 v[160:163], a[24:27], v[244:247], v[160:163]\n"
    "v_mfma_f32_16x16x32_bf16 v[160:163], a[16:19], v[240:243], v[160:163]\n"
    "v_mfma_f32_16x16x32_bf16 v[160:163], a[8:11], v[236:239], v[160:163]\n"
    "v_mfma_f32_16x16x32_bf16 v[160:163], a[0:3], v[232:235], v[160:163]\n"
    "s_waitcnt lgkmcnt(3)\n"
    "ds_read_b128 v[232:235], v25 offset:25600\n"
    "s_add_u32 m0, s57, 106496\n"
    "v_mfma_f32_16x16x32_bf16 v[168:171], a[0:3], a[208:211], v[168:171]\n"
    "ds_read_b128 v[236:239], v26 offset:25600\n"
    "v_add_u32_dpp v17, v10, v2 row_newbcast:1 row_mask:0xf bank_mask:0xf\n"
    "v_mfma_f32_16x16x32_bf16 v[168:171], a[8:11], a[212:215], v[168:171]\n"
    "ds_read_b128 v[240:243], v27 offset:25600\n"
    "v_mfma_f32_16x16x32_bf16 v[168:171], a[16:19], a[216:219], v[168:171]\n"
    "ds_read_b128 v[244:247], v28 offset:25600\n"
    "v_mfma_f32_16x16x32_bf16 v[168:171], a[24:27], a[220:223], v[168:171]\n"
    "buffer_load_dwordx4 v17, s[48:51], 0 offen offset:0 lds\n"
    "v_add_u32_dpp v18, v10, v3 row_newbcast:9 row_mask:0xf bank_mask:0xf\n"
    "v_mfma_f32_16x16x32_bf16 v[172:175], a[28:31], a[220:223], v[172:175]\n"
    "v_mfma_f32_16x16x32_bf16 v[172:175], a[20:23], a[216:219], v[172:175]\n"
    "v_mfma_f32_16x16x32_bf16 v[172:175], a[12:15], a[212:215], v[172:175]\n"
    "v_mfma_f32_16x16x32_bf16 v[172:175], a[4:7], a[208:211], v[172:175]\n"
    "v_mfma_f32_16x16x32_bf16 v[180:183], a[4:7], a[224:227], v[180:183]\n"
    "v_mfma_f32_16x16x32_bf16 v[180:183], a[12:15], a[228:231], v[180:183]\n"
    "v_mfma_f32_16x16x32_bf16 v[180:183], a[20:23], a[232:235], v[180:183]\n"
    "v_mfma_f32_16x16x32_bf16 v[180:183], a[28:31], a[236:239], v[180:183]\n"
    "v_mfma_f32_16x16x32_bf16 v[176:179], a[24:27], a[236:239], v[176:179]\n"
    "buffer_load_dwordx4 v18, s[48:51], 0 offen offset:1024 lds\n"
    "s_add_u32 m0, s57, 155648\n"
    "v_mfma_f32_16x16x32_bf16 v[176:179], a[16:19], a[232:235], v[176:179]\n"
    "v_add_u32_dpp v19, v10, v2 row_newbcast:5 row_mask:0xf bank_mask:0xf\n"
    "v_mfma_f32_16x16x32_bf16 v[176:179], a[8:11], a[228:231], v[176:179]\n"
    "v_mfma_f32_16x16x32_bf16 v[176:179], a[0:3], a[224:227], v[176:179]\n"
    "s_waitcnt vmcnt(8) lgkmcnt(0)\n"
    "s_barrier\n"
    "v_mfma_f32_16x16x32_bf16 v[184:187], a[0:3], a[240:243], v[184:187]\n"
    "ds_read_b128 a[208:211], v25 offset:8192\n"
    "buffer_load_dwordx4 v19, s[48:51], 0 offen offset:0 lds\n"
    "v_mfma_f32_16x16x32_bf16 v[184:187], a[8:11], a[244:247], v[184:187]\n"
    "ds_read_b128 a[212:215], v26 offset:8192\n"
    "v_add_u32_dpp v20, v10, v3 row_newbcast:13 row_mask:0xf bank_mask:0xf\n"
    "v_mfma_f32_16x16x32_bf16 v[184:187], a[16:19], a[248:251], v[184:187]\n"
    "ds_read_b128 a[216:219], v27 offset:8192\n"
    "v_mfma_f32_16x16x32_bf16 v[184:187], a[24:27], a[252:255], v[184:187]\n"
    "ds_read_b128 a[220:223], v28 offset:8192\n"
    "buffer_load_dwordx4 v20, s[48:51], 0 offen offset:1024 lds\n"
    "v_mfma_f32_16x16x32_bf16 v[188:191], a[28:31], a[252:255], v[188:191]\n"
    "s_add_u32 m0, s57, 114688\n"
    "ds_read_b128 a[224:227], v25 offset:9216\n"
    "v_add_u32_dpp v17, v10, v4 row_newbcast:6 row_mask:0x5 bank_mask:0x3\n"
    "v_mfma_f32_16x16x32_bf16 v[188:191], a[20:23], a[248:251], v[188:191]\n"
    "ds_read_b128 a[228:231], v26 offset:9216\n"
    "v_add_u32_dpp v17, v10, v4 row_newbcast:6 row_mask:0xa bank_mask:0xc\n"
    "v_mfma_f32_16x16x32_bf16 v[188:191], a[12:15], a[244:247], v[188:191]\n"
    "ds_read_b128 a[232:235], v27 offset:9216\n"
    "v_add_u32_dpp v17, v12, v4 row_newbcast:6 row_mask:0x5 bank_mask:0xc\n"
    "v_mfma_f32_16x16x32_bf16 v[188:191], a[4:7], a[240:243], v[188:191]\n"
    "ds_read_b128 a[236:239], v28 offset:9216\n"
    "v_add_u32_dpp v17, v12, v4 row_newbcast:6 row_mask:0xa bank_mask:0x3\n"
    "v_mfma_f32_16x16x32_bf16 v[196:199], a[4:7], v[232:235], v[196:199]\n"
    "ds_read_b128 a[240:243], v25 offset:0\n"
    "buffer_load_dwordx4 v17, s[48:51], 0 offen offset:0 lds\n"
    "v_mfma_f32_16x16x32_bf16 v[196:199], a[12:15], v[236:239], v[196:199]\n"
    "ds_read_b128 a[244:247], v26 offset:0\n"
    "v_add_u32_dpp v18, v14, v5 row_newbcast:6 row_mask:0x5 bank_mask:0x3\n"
    "v_mfma_f32_16x16x32_bf16 v[196:199], a[20:23], v[240:243], v[196:199]\n"
    "ds_read_b128 a[248:251], v27 offset:0\n"
    "v_add_u32_dpp v18, v14, v5 row_newbcast:6 row_mask:0xa bank_mask:0xc\n"
    "v_mfma_f32_16x16x32_bf16 v[196:199], a[28:31], v[244:247], v[196:199]\n"
    "ds_read_b128 a[252:255], v28 offset:0\n"
    "v_add_u32_dpp v18, v16, v5 row_newbcast:6 row_mask:0x5 bank_mask:0xc\n"
    "v_mfma_f32_16x16x32_bf16 v[192:195], a[24:27], v[244:247], v[192:195]\n"
    "v_add_u32_dpp v18, v16, v5 row_newbcast:6 row_mask:0xa bank_mask:0x3\n"
    "v_mfma_f32_16x16x32_bf16 v[192:195], a[16:19], v[240:243], v[192:195]\n"
    "buffer_load_dwordx4 v18, s[48:51], 0 offen offset:1024 lds\n"
    "s_add_u32 m0, s57, 122880\n"
    "v_mfma_f32_16x16x32_bf16 v[192:195], a[8:11], v[236:239], v[192:195]\n"
    "v_add_u32_dpp v19, v10, v4 row_newbcast:14 row_mask:0x5 bank_mask:0x3\n"
    "v_add_u32_dpp v19, v10, v4 row_newbcast:14 row_mask:0xa bank_mask:0xc\n"
    "v_mfma_f32_16x16x32_bf16 v[192:195], a[0:3], v[232:235], v[192:195]\n"
    "s_waitcnt lgkmcnt(3)\n"
    "ds_read_b128 v[232:235], v25 offset:1024\n"
    "v_add_u32_dpp v19, v12, v4 row_newbcast:14 row_mask:0x5 bank_mask:0xc\n"
    "v_mfma_f32_16x16x32_bf16 v[168:171], a[128:131], a[208:211], v[168:171]\n"
    "ds_read_b128 v[236:239], v26 offset:1024\n"
    "v_add_u32_dpp v19, v12, v4 row_newbcast:14 row_mask:0xa bank_mask:0x3\n"
    "v_mfma_f32_16x16x32_bf16 v[168:171], a[136:139], a[212:215], v[168:171]\n"
    "ds_read_b128 v[240:243], v27 offset:1024\n"
    "buffer_load_dwordx4 v19, s[48:51], 0 offen offset:0 lds\n"
    "v_mfma_f32_16x16x32_bf16 v[168:171], a[144:147], a[216:219], v[168:171]\n"
    "ds_read_b128 v[244:247], v28 offset:1024\n"
    "v_add_u32_dpp v20, v14, v5 row_newbcast:14 row_mask:0x5 bank_mask:0x3\n"
    "v_mfma_f32_16x16x32_bf16 v[168:171], a[152:155], a[220:223], v[168:171]\n"
    "v_add_u32_dpp v20, v14, v5 row_newbcast:14 row_mask:0xa bank_mask:0xc\n"
    "v_add_u32_dpp v20, v16, v5 row_newbcast:14 row_mask:0x5 bank_mask:0xc\n"
    "v_mfma_f32_16x16x32_bf16 v[172:175], a[156:159], a[220:223], v[172:175]\n"
    "v_add_u32_dpp v20, v16, v5 row_newbcast:14 row_mask:0xa bank_mask:0x3\n"
    "v_mfma_f32_16x16x32_bf16 v[172:175], a[148:151], a[216:219], v[172:175]\n"
    "buffer_load_dwordx4 v20, s[48:51], 0 offen offset:1024 lds\n"
    "s_add_u32 m0, s57, 131072\n"
    "v_mfma_f32_16x16x32_bf16 v[172:175], a[140:143], a[212:215], v[172:175]\n"
    "v_add_u32_dpp v17, v11, v2 row_newbcast:1 row_mask:0xf bank_mask:0xf\n"
    "v_mfma_f32_16x16x32_bf16 v[172:175], a[132:135], a[208:211], v[172:175]\n"
    "v_mfma_f32_16x16x32_bf16 v[180:183], a[132:135], a[224:227], v[180:183]\n"
    "buffer_load_dwordx4 v17, s[48:51], 0 offen offset:0 lds\n"
    "v_add_u32_dpp v18, v11, v3 row_newbcast:9 row_mask:0xf bank_mask:0xf\n"
    "v_mfma_f32_16x16x32_bf16 v[180:183], a[140:143], a[228:231], v[180:183]\n"
    "v_mfma_f32_16x16x32_bf16 v[180:183], a[148:151], a[232:235], v[180:183]\n"
    "v_mfma_f32_16x16x32_bf16 v[180:183], a[156:159], a[236:239], v[180:183]\n"
    "v_mfma_f32_16x16x32_bf16 v[176:179], a[152:155], a[236:239], v[176:179]\n"
    "v_mfma_f32_16x16x32_bf16 v[176:179], a[144:147], a[232:235], v[176:179]\n"
    "buffer_load_dwordx4 v18, s[48:51], 0 offen offset:1024 lds\n"
    "s_add_u32 m0, s57, 139264\n"
    "v_mfma_f32_16x16x32_bf16 v[176:179], a[136:139], a[228:231], v[176:179]\n"
    "v_add_u32_dpp v19, v12, v2 row_newbcast:1 row_mask:0xf bank_mask:0xf\n"
    "v_mfma_f32_16x16x32_bf16 v[176:179], a[128:131], a[224:227], v[176:179]\n"
    "s_waitcnt vmcnt(10) lgkmcnt(0)\n"
    "s_barrier\n"
    "v_mfma_f32_16x16x32_bf16 v[168:171], a[160:163], a[240:243], v[168:171]\n"
    "ds_read_b128 a[208:211], v25 offset:16384\n"
    "buffer_load_dwordx4 v19, s[48:51], 0 offen offset:0 lds\n"
    "v_mfma_f32_16x16x32_bf16 v[168:171], a[168:171], a[244:247], v[168:171]\n"
    "ds_read_b128 a[212:215], v26 offset:16384\n"
    "v_add_u32_dpp v20, v12, v3 row_newbcast:9 row_mask:0xf bank_mask:0xf\n"
    "v_mfma_f32_16x16x32_bf16 v[168:171], a[176:179], a[248:251], v[168:171]\n"
    "ds_read_b128 a[216:219], v27 offset:16384\n"
    "v_mfma_f32_16x16x32_bf16 v[168:171], a[184:187], a[252:255], v[168:171]\n"
    "ds_read_b64_tr_b16 a[56:57], v49 offset:16384\n"
    "buffer_load_dwordx4 v20, s[48:51], 0 offen offset:1024 lds\n"
    "v_mfma_f32_16x16x32_bf16 v[172:175], a[188:191], a[252:255], v[172:175]\n"
    "s_add_u32 m0, s57, 0\n"
    "ds_read_b64_tr_b16 a[58:59], v49 offset:16896\n"
    "v_add_u32_dpp v17, v12, v2 row_newbcast:4 row_mask:0xf bank_mask:0xf\n"
    "v_mfma_f32_16x16x32_bf16 v[172:175], a[180:183], a[248:251], v[172:175]\n"
    "ds_read_b128 a[220:223], v28 offset:16384\n"
    "v_mfma_f32_16x16x32_bf16 v[172:175], a[172:175], a[244:247], v[172:175]\n"
    "ds_read_b64_tr_b16 a[60:61], v50 offset:16384\n"
    "v_mfma_f32_16x16x32_bf16 v[172:175], a[164:167], a[240:243], v[172:175]\n"
    "ds_read_b64_tr_b16 a[62:63], v50 offset:16896\n"
    "buffer_load_dwordx4 v17, s[48:51], 0 offen offset:0 lds\n"
    "v_mfma_f32_16x16x32_bf16 v[180:183], a[164:167], v[232:235], v[180:183]\n"
    "ds_read_b128 a[224:227], v25 offset:17408\n"
    "v_add_u32_dpp v18, v12, v3 row_newbcast:12 row_mask:0xf bank_mask:0xf\n"
    "v_mfma_f32_16x16x32_bf16 v[180:183], a[172:175], v[236:239], v[180:183]\n"
    "ds_read_b128 a[228:231], v26 offset:17408\n"
    "v_mfma_f32_16x16x32_bf16 v[180:183], a[180:183], v[240:243], v[180:183]\n"
    "ds_read_b128 a[232:235], v27 offset:17408\n"
    "v_mfma_f32_16x16x32_bf16 v[180:183], a[188:191], v[244:247], v[180:183]\n"
    "ds_read_b128 a[236:239], v28 offset:17408\n"
    "v_mfma_f32_16x16x32_bf16 v[176:179], a[184:187], v[244:247], v[176:179]\n"
    "ds_read_b128 a[240:243], v25 offset:32768\n"
    "buffer_load_dwordx4 v18, s[48:51], 0 offen offset:1024 lds\n"
    "v_mfma_f32_16x16x32_bf16 v[176:179], a[176:179], v[240:243], v[176:179]\n"
    "s_add_u32 m0, s57, 40960\n"
    "ds_read_b128 a[244:247], v26 offset:32768\n"
    "v_add_u32_dpp v19, v12, v2 row_newbcast:5 row_mask:0xf bank_mask:0xf\n"
    "v_mfma_f32_16x16x32_bf16 v[176:179], a[168:171], v[236:239], v[176:179]\n"
    "ds_read_b128 a[248:251], v27 offset:32768\n"
    "v_mfma_f32_16x16x32_bf16 v[176:179], a[160:163], v[232:235], v[176:179]\n"
    "s_waitcnt lgkmcnt(6)\n"
    "ds_read_b128 a[252:255], v28 offset:32768\n"
    "v_mfma_f32_16x16x32_bf16 v[104:107], a[32:35], a[208:211], v[104:107]\n"
    "ds_read_b128 v[232:235], v25 offset:33792\n"
    "buffer_load_dwordx4 v19, s[48:51], 0 offen offset:0 lds\n"
    "v_mfma_f32_16x16x32_bf16 v[104:107], a[40:43], a[212:215], v[104:107]\n"
    "ds_read_b128 v[236:239], v26 offset:33792\n"
    "v_add_u32_dpp v20, v12, v3 row_newbcast:13 row_mask:0xf bank_mask:0xf\n"
    "v_mfma_f32_16x16x32_bf16 v[104:107], a[48:51], a[216:219], v[104:107]\n"
    "ds_read_b128 v[240:243], v27 offset:33792\n"
    "v_mfma_f32_16x16x32_bf16 v[104:107], a[56:59], a[220:223], v[104:107]\n"
    "ds_read_b128 v[244:247], v28 offset:33792\n"
    "v_mfma_f32_16x16x32_bf16 v[108:111], a[60:63], a[220:223], v[108:111]\n"
    "buffer_load_dwordx4 v20, s[48:51], 0 offen offset:1024 lds\n"
    "s_add_u32 m0, s57, 24576\n"
    "v_mfma_f32_16x16x32_bf16 v[108:111], a[52:55], a[216:219], v[108:111]\n"
    "v_add_u32_dpp v17, v13, v2 row_newbcast:1 row_mask:0xf bank_mask:0xf\n"
    "v_mfma_f32_16x16x32_bf16 v[108:111], a[44:47], a[212:215], v[108:111]\n"
    "v_mfma_f32_16x16x32_bf16 v[108:111], a[36:39], a[208:211], v[108:111]\n"
    "v_mfma_f32_16x16x32_bf16 v[116:119], a[36:39], a[224:227], v[116:119]\n"
    "s_waitcnt lgkmcnt(2)\n"
    "v_mfma_f32_16x16x32_bf16 v[116:119], a[44:47], a[228:231], v[116:119]\n"
    "buffer_load_dwordx4 v17, s[48:51], 0 offen offset:0 lds\n"
    "v_add_u32_dpp v18, v13, v3 row_newbcast:9 row_mask:0xf bank_mask:0xf\n"
    "v_mfma_f32_16x16x32_bf16 v[116:119], a[52:55], a[232:235], v[116:119]\n"
    "v_mfma_f32_16x16x32_bf16 v[116:119], a[60:63], a[236:239], v[116:119]\n"
    "v_mfma_f32_16x16x32_bf16 v[112:115], a[56:59], a[236:239], v[112:115]\n"
    "v_cvt_pk_bf16_f32 v104, v104, v105\n"
    "v_cvt_pk_bf16_f32 v105, v106, v107\n"
    "v_mfma_f32_16x16x32_bf16 v[112:115], a[48:51], a[232:235], v[112:115]\n"
    "buffer_load_dwordx4 v18, s[48:51], 0 offen offset:1024 lds\n"
    "s_add_u32 m0, s57, 8192\n"
    "v_mfma_f32_16x16x32_bf16 v[112:115], a[40:43], a[228:231], v[112:115]\n"
    "v_add_u32_dpp v19, v14, v2 row_newbcast:1 row_mask:0xf bank_mask:0xf\n"
    "v_cvt_pk_bf16_f32 v106, v108, v109\n"
    "v_mfma_f32_16x16x32_bf16 v[112:115], a[32:35], a[224:227], v[112:115]\n"
    "s_waitcnt vmcnt(14) lgkmcnt(0)\n"
    "s_barrier\n"
    "v_mfma_f32_16x16x32_bf16 v[120:123], a[128:131], a[240:243], v[120:123]\n"
    "ds_read_b128 a[208:211], v25 offset:40960\n"
    "buffer_load_dwordx4 v19, s[48:51], 0 offen offset:0 lds\n"
    "v_mfma_f32_16x16x32_bf16 v[120:123], a[136:139], a[244:247], v[120:123]\n"
    "ds_read_b128 a[212:215], v26 offset:40960\n"
    "v_add_u32_dpp v20, v14, v3 row_newbcast:9 row_mask:0xf bank_mask:0xf\n"
    "v_mfma_f32_16x16x32_bf16 v[120:123], a[144:147], a[248:251], v[120:123]\n"
    "ds_read_b128 a[216:219], v27 offset:40960\n"
    "v_cvt_pk_bf16_f32 v107, v110, v111\n"
    "v_mfma_f32_16x16x32_bf16 v[120:123], a[152:155], a[252:255], v[120:123]\n"
    "buffer_store_dwordx4 v[104:107], v53, s[52:55], s34 offen offset:0 sc1\n"
    "v_mfma_f32_16x16x32_bf16 v[124:127], a[156:159], a[252:255], v[124:127]\n"
    "ds_read_b128 a[220:223], v28 offset:40960\n"
    "v_cvt_pk_bf16_f32 v112, v112, v113\n"
    "v_mfma_f32_16x16x32_bf16 v[124:127], a[148:151], a[248:251], v[124:127]\n"
    "ds_read_b128 a[224:227], v25 offset:41984\n"
    "v_cvt_pk_bf16_f32 v113, v114, v115\n"
    "v_mfma_f32_16x16x32_bf16 v[124:127], a[140:143], a[244:247], v[124:127]\n"
    "ds_read_b128 a[228:231], v26 offset:41984\n"
    "v_cvt_pk_bf16_f32 v114, v116, v117\n"
    "v_mfma_f32_16x16x32_bf16 v[124:127], a[132:135], a[240:243], v[124:127]\n"
    "ds_read_b128 a[232:235], v27 offset:41984\n"
    "v_cvt_pk_bf16_f32 v115, v118, v119\n"
    "v_mfma_f32_16x16x32_bf16 v[132:135], a[132:135], v[232:235], v[132:135]\n"
    "ds_read_b128 a[236:239], v28 offset:41984\n"
    "v_mfma_f32_16x16x32_bf16 v[132:135], a[140:143], v[236:239], v[132:135]\n"
    "ds_read_b128 a[240:243], v29 offset:24576\n"
    "v_mfma_f32_16x16x32_bf16 v[132:135], a[148:151], v[240:243], v[132:135]\n"
    "ds_read_b128 a[244:247], v30 offset:24576\n"
    "v_mfma_f32_16x16x32_bf16 v[132:135], a[156:159], v[244:247], v[132:135]\n"
    "ds_read_b128 a[248:251], v31 offset:24576\n"
    "v_mfma_f32_16x16x32_bf16 v[128:131], a[152:155], v[244:247], v[128:131]\n"
    "ds_read_b128 a[252:255], v32 offset:24576\n"
    "v_mfma_f32_16x16x32_bf16 v[128:131], a[144:147], v[240:243], v[128:131]\n"
    "buffer_load_dwordx4 v20, s[48:51], 0 offen offset:1024 lds\n"
    "s_add_u32 m0, s57, 16384\n"
    "v_mfma_f32_16x16x32_bf16 v[128:131], a[136:139], v[236:239], v[128:131]\n"
    "v_add_u32_dpp v17, v14, v2 row_newbcast:4 row_mask:0xf bank_mask:0xf\n"
    "v_mfma_f32_16x16x32_bf16 v[128:131], a[128:131], v[232:235], v[128:131]\n"
    "s_waitcnt lgkmcnt(4)\n"
    "ds_read_b128 v[232:235], v29 offset:25600\n"
    "buffer_load_dwordx4 v17, s[48:51], 0 offen offset:0 lds\n"
    "v_mfma_f32_16x16x32_bf16 v[120:123], a[32:35], a[208:211], v[120:123]\n"
    "ds_read_b128 v[236:239], v30 offset:25600\n"
    "v_add_u32_dpp v18, v14, v3 row_newbcast:12 row_mask:0xf bank_mask:0xf\n"
    "v_mfma_f32_16x16x32_bf16 v[120:123], a[40:43], a[212:215], v[120:123]\n"
    "buffer_store_dwordx4 v[112:115], v53, s[52:55], s34 offen offset:1024 sc1\n"
    "v_mfma_f32_16x16x32_bf16 v[120:123], a[48:51], a[216:219], v[120:123]\n"
    "ds_read_b128 v[240:243], v31 offset:25600\n"
    "v_mfma_f32_16x16x32_bf16 v[120:123], a[56:59], a[220:223], v[120:123]\n"
    "ds_read_b128 v[244:247], v32 offset:25600\n"
    "v_mfma_f32_16x16x32_bf16 v[124:127], a[60:63], a[220:223], v[124:127]\n"
    "v_mfma_f32_16x16x32_bf16 v[124:127], a[52:55], a[216:219], v[124:127]\n"
    "v_mfma_f32_16x16x32_bf16 v[124:127], a[44:47], a[212:215], v[124:127]\n"
    "v_mfma_f32_16x16x32_bf16 v[124:127], a[36:39], a[208:211], v[124:127]\n"
    "v_mfma_f32_16x16x32_bf16 v[132:135], a[36:39], a[224:227], v[132:135]\n"
    "v_mfma_f32_16x16x32_bf16 v[132:135], a[44:47], a[228:231], v[132:135]\n"
    "v_mfma_f32_16x16x32_bf16 v[132:135], a[52:55], a[232:235], v[132:135]\n"
    "v_mfma_f32_16x16x32_bf16 v[132:135], a[60:63], a[236:239], v[132:135]\n"
    "v_mfma_f32_16x16x32_bf16 v[128:131], a[56:59], a[236:239], v[128:131]\n"
    "v_mfma_f32_16x16x32_bf16 v[128:131], a[48:51], a[232:235], v[128:131]\n"
    "v_mfma_f32_16x16x32_bf16 v[128:131], a[40:43], a[228:231], v[128:131]\n"
    "v_mfma_f32_16x16x32_bf16 v[128:131], a[32:35], a[224:227], v[128:131]\n"
    "s_waitcnt vmcnt(15) lgkmcnt(0)\n"
    "s_barrier\n"
    "v_mfma_f32_16x16x32_bf16 v[120:123], a[160:163], a[240:243], v[120:123]\n"
    "ds_read_b128 a[208:211], v37 offset:49152\n"
    "buffer_load_dwordx4 v18, s[48:51], 0 offen offset:1024 lds\n"
    "v_mfma_f32_16x16x32_bf16 v[120:123], a[168:171], a[244:247], v[120:123]\n"
    "s_add_u32 m0, s57, 49152\n"
    "ds_read_b128 a[212:215], v38 offset:49152\n"
    "v_add_u32_dpp v19, v14, v2 row_newbcast:5 row_mask:0xf bank_mask:0xf\n"
    "v_mfma_f32_16x16x32_bf16 v[120:123], a[176:179], a[248:251], v[120:123]\n"
    "ds_read_b128 a[216:219], v39 offset:49152\n"
    "buffer_load_dwordx4 v19, s[48:51], 0 offen offset:0 lds\n"
    "v_mfma_f32_16x16x32_bf16 v[120:123], a[184:187], a[252:255], v[120:123]\n"
    "ds_read_b128 a[220:223], v40 offset:49152\n"
    "v_add_u32_dpp v20, v14, v3 row_newbcast:13 row_mask:0xf bank_mask:0xf\n"
    "v_mfma_f32_16x16x32_bf16 v[124:127], a[188:191], a[252:255], v[124:127]\n"
    "ds_read_b128 a[224:227], v37 offset:50176\n"
    "buffer_load_dwordx4 v20, s[48:51], 0 offen offset:1024 lds\n"
    "v_mfma_f32_16x16x32_bf16 v[124:127], a[180:183], a[248:251], v[124:127]\n"
    "ds_read_b128 a[228:231], v38 offset:50176\n"
    "v_mfma_f32_16x16x32_bf16 v[124:127], a[172:175], a[244:247], v[124:127]\n"
    "ds_read_b128 a[232:235], v39 offset:50176\n"
    "v_mfma_f32_16x16x32_bf16 v[124:127], a[164:167], a[240:243], v[124:127]\n"
    "ds_read_b128 a[236:239], v40 offset:50176\n"
    "v_mfma_f32_16x16x32_bf16 v[132:135], a[164:167], v[232:235], v[132:135]\n"
    "ds_read_b128 a[240:243], v37 offset:57344\n"
    "v_mfma_f32_16x16x32_bf16 v[132:135], a[172:175], v[236:239], v[132:135]\n"
    "ds_read_b128 a[244:247], v38 offset:57344\n"
    "v_mfma_f32_16x16x32_bf16 v[132:135], a[180:183], v[240:243], v[132:135]\n"
    "ds_read_b128 a[248:251], v39 offset:57344\n"
    "v_mfma_f32_16x16x32_bf16 v[132:135], a[188:191], v[244:247], v[132:135]\n"
    "ds_read_b128 a[252:255], v40 offset:57344\n"
    "v_mfma_f32_16x16x32_bf16 v[128:131], a[184:187], v[244:247], v[128:131]\n"
    "v_mfma_f32_16x16x32_bf16 v[128:131], a[176:179], v[240:243], v[128:131]\n"
    "v_mfma_f32_16x16x32_bf16 v[128:131], a[168:171], v[236:239], v[128:131]\n"
    "v_mfma_f32_16x16x32_bf16 v[128:131], a[160:163], v[232:235], v[128:131]\n"
    "s_waitcnt lgkmcnt(3)\n"
    "ds_read_b128 v[232:235], v37 offset:58368\n"
    "v_mfma_f32_16x16x32_bf16 v[120:123], a[192:195], a[208:211], v[120:123]\n"
    "ds_read_b128 v[236:239], v38 offset:58368\n"
    "v_mfma_f32_16x16x32_bf16 v[120:123], a[200:203], a[212:215], v[120:123]\n"
    "ds_read_b128 v[240:243], v39 offset:58368\n"
    "v_mfma_f32_16x16x32_bf16 v[124:127], a[204:207], a[212:215], v[124:127]\n"
    "ds_read_b128 v[244:247], v40 offset:58368\n"
    "v_mfma_f32_16x16x32_bf16 v[124:127], a[196:199], a[208:211], v[124:127]\n"
    "v_mfma_f32_16x16x32_bf16 v[156:159], a[196:199], a[216:219], v[156:159]\n"
    "v_mfma_f32_16x16x32_bf16 v[156:159], a[204:207], a[220:223], v[156:159]\n"
    "v_mfma_f32_16x16x32_bf16 v[152:155], a[200:203], a[220:223], v[152:155]\n"
    "v_mfma_f32_16x16x32_bf16 v[152:155], a[192:195], a[216:219], v[152:155]\n"
    "v_mfma_f32_16x16x32_bf16 v[184:187], a[192:195], a[224:227], v[184:187]\n"
    "v_mfma_f32_16x16x32_bf16 v[184:187], a[200:203], a[228:231], v[184:187]\n"
    "v_mfma_f32_16x16x32_bf16 v[188:191], a[204:207], a[228:231], v[188:191]\n"
    "v_mfma_f32_16x16x32_bf16 v[188:191], a[196:199], a[224:227], v[188:191]\n"
    "s_add_u32 m0, s57, 32768\n"
    "v_add_u32_dpp v17, v15, v2 row_newbcast:2 row_mask:0xf bank_mask:0xf\n"
    "v_cvt_pk_bf16_f32 v120, v120, v121\n"
    "v_mfma_f32_16x16x32_bf16 v[220:223], a[196:199], a[232:235], 0\n"
    "v_cvt_pk_bf16_f32 v121, v122, v123\n"
    "v_cvt_pk_bf16_f32 v122, v124, v125\n"
    "v_mfma_f32_16x16x32_bf16 v[220:223], a[204:207], a[236:239], v[220:223]\n"
    "v_cvt_pk_bf16_f32 v123, v126, v127\n"
    "v_mfma_f32_16x16x32_bf16 v[216:219], a[200:203], a[236:239], 0\n"
    "buffer_store_dwordx4 v[120:123], v53, s[52:55], s34 offen offset:2048 sc1\n"
    "v_mfma_f32_16x16x32_bf16 v[216:219], a[192:195], a[232:235], v[216:219]\n"
    "s_waitcnt vmcnt(15) lgkmcnt(0)\n"
    "s_barrier\n"
    "v_mfma_f32_16x16x32_bf16 v[128:131], a[192:195], a[240:243], v[128:131]\n"
    "ds_read_b128 a[208:211], v29 offset:0\n"
    "v_mfma_f32_16x16x32_bf16 v[128:131], a[200:203], a[244:247], v[128:131]\n"
    "ds_read_b128 a[212:215], v30 offset:0\n"
    "v_mfma_f32_16x16x32_bf16 v[132:135], a[204:207], a[244:247], v[132:135]\n"
    "ds_read_b128 a[216:219], v31 offset:0\n"
    "v_mfma_f32_16x16x32_bf16 v[132:135], a[196:199], a[240:243], v[132:135]\n"
    "ds_read_b128 a[220:223], v32 offset:0\n"
    "v_mfma_f32_16x16x32_bf16 v[164:167], a[196:199], a[248:251], v[164:167]\n"
    "ds_read_b128 a[224:227], v29 offset:1024\n"
    "v_mfma_f32_16x16x32_bf16 v[164:167], a[204:207], a[252:255], v[164:167]\n"
    "ds_read_b128 a[228:231], v30 offset:1024\n"
    "v_mfma_f32_16x16x32_bf16 v[160:163], a[200:203], a[252:255], v[160:163]\n"
    "ds_read_b128 a[232:235], v31 offset:1024\n"
    "buffer_load_dwordx4 v17, s[48:51], 0 offen offset:0 lds\n"
    "v_mfma_f32_16x16x32_bf16 v[160:163], a[192:195], a[248:251], v[160:163]\n"
    "ds_read_b128 a[236:239], v32 offset:1024\n"
    "v_add_u32_dpp v18, v15, v3 row_newbcast:10 row_mask:0xf bank_mask:0xf\n"
    "v_mfma_f32_16x16x32_bf16 v[192:195], a[192:195], v[232:235], v[192:195]\n"
    "ds_read_b128 a[240:243], v29 offset:8192\n"
    "buffer_load_dwordx4 v18, s[48:51], 0 offen offset:1024 lds\n"
    "v_mfma_f32_16x16x32_bf16 v[192:195], a[200:203], v[236:239], v[192:195]\n"
    "s_add_u32 m0, s57, 57344\n"
    "ds_read_b128 a[244:247], v30 offset:8192\n"
    "v_add_u32_dpp v19, v16, v2 row_newbcast:3 row_mask:0xf bank_mask:0xf\n"
    "v_mfma_f32_16x16x32_bf16 v[196:199], a[204:207], v[236:239], v[196:199]\n"
    "ds_read_b128 a[248:251], v31 offset:8192\n"
    "buffer_load_dwordx4 v19, s[48:51], 0 offen offset:0 lds\n"
    "v_mfma_f32_16x16x32_bf16 v[196:199], a[196:199], v[232:235], v[196:199]\n"
    "ds_read_b128 a[252:255], v32 offset:8192\n"
    "v_add_u32_dpp v20, v16, v3 row_newbcast:11 row_mask:0xf bank_mask:0xf\n"
    "v_mfma_f32_16x16x32_bf16 v[228:231], a[196:199], v[240:243], 0\n"
    "buffer_load_dwordx4 v20, s[48:51], 0 offen offset:1024 lds\n"
    "s_add_u32 m0, s57, 90112\n"
    "v_mfma_f32_16x16x32_bf16 v[228:231], a[204:207], v[244:247], v[228:231]\n"
    "v_add_u32_dpp v17, v16, v2 row_newbcast:2 row_mask:0xf bank_mask:0xf\n"
    "v_cvt_pk_bf16_f32 v128, v128, v129\n"
    "v_mfma_f32_16x16x32_bf16 v[224:227], a[200:203], v[244:247], 0\n"
    "buffer_load_dwordx4 v17, s[48:51], 0 offen offset:0 lds\n"
    "v_add_u32_dpp v18, v16, v3 row_newbcast:10 row_mask:0xf bank_mask:0xf\n"
    "v_mfma_f32_16x16x32_bf16 v[224:227], a[192:195], v[240:243], v[224:227]\n"
    "s_waitcnt lgkmcnt(3)\n"
    "ds_read_b128 v[232:235], v29 offset:9216\n"
    "buffer_load_dwordx4 v18, s[48:51], 0 offen offset:1024 lds\n"
    "v_mfma_f32_16x16x32_bf16 v[136:139], a[32:35], a[208:211], v[136:139]\n"
    "s_add_u32 m0, s57, 73728\n"
    "ds_read_b128 v[236:239], v30 offset:9216\n"
    "v_add_u32_dpp v19, v15, v2 row_newbcast:3 row_mask:0xf bank_mask:0xf\n"
    "v_mfma_f32_16x16x32_bf16 v[136:139], a[40:43], a[212:215], v[136:139]\n"
    "ds_read_b128 v[240:243], v31 offset:9216\n"
    "buffer_load_dwordx4 v19, s[48:51], 0 offen offset:0 lds\n"
    "v_mfma_f32_16x16x32_bf16 v[136:139], a[48:51], a[216:219], v[136:139]\n"
    "ds_read_b128 v[244:247], v32 offset:9216\n"
    "v_add_u32_dpp v20, v15, v3 row_newbcast:11 row_mask:0xf bank_mask:0xf\n"
    "v_mfma_f32_16x16x32_bf16 v[136:139], a[56:59], a[220:223], v[136:139]\n"
    "buffer_load_dwordx4 v20, s[48:51], 0 offen offset:1024 lds\n"
    "v_cvt_pk_bf16_f32 v129, v130, v131\n"
    "v_mfma_f32_16x16x32_bf16 v[140:143], a[60:63], a[220:223], v[140:143]\n"
    "v_cvt_pk_bf16_f32 v130, v132, v133\n"
    "v_cvt_pk_bf16_f32 v131, v134, v135\n"
    "v_mfma_f32_16x16x32_bf16 v[140:143], a[52:55], a[216:219], v[140:143]\n"
    "buffer_store_dwordx4 v[128:131], v53, s[52:55], s34 offen offset:3072 sc1\n"
    "v_mfma_f32_16x16x32_bf16 v[140:143], a[44:47], a[212:215], v[140:143]\n"
    "v_pk_add_f32 v[200:201], v[200:201], v[84:85]\n"
    "v_pk_add_f32 v[202:203], v[202:203], v[86:87]\n"
    "v_mfma_f32_16x16x32_bf16 v[140:143], a[36:39], a[208:211], v[140:143]\n"
    "v_pk_add_f32 v[204:205], v[204:205], v[88:89]\n"
    "v_pk_add_f32 v[206:207], v[206:207], v[90:91]\n"
    "v_mfma_f32_16x16x32_bf16 v[148:151], a[36:39], a[224:227], v[148:151]\n"
    "v_pk_add_f32 v[208:209], v[208:209], v[84:85]\n"
    "v_pk_add_f32 v[210:211], v[210:211], v[86:87]\n"
    "v_mfma_f32_16x16x32_bf16 v[148:151], a[44:47], a[228:231], v[148:151]\n"
    "v_pk_add_f32 v[212:213], v[212:213], v[88:89]\n"
    "v_pk_add_f32 v[214:215], v[214:215], v[90:91]\n"
    "v_mfma_f32_16x16x32_bf16 v[148:151], a[52:55], a[232:235], v[148:151]\n"
    "v_cvt_pk_bf16_f32 v136, v136, v137\n"
    "v_cvt_pk_bf16_f32 v137, v138, v139\n"
    "v_mfma_f32_16x16x32_bf16 v[148:151], a[60:63], a[236:239], v[148:151]\n"
    "v_cvt_pk_bf16_f32 v138, v140, v141\n"
    "v_cvt_pk_bf16_f32 v139, v142, v143\n"
    "v_mfma_f32_16x16x32_bf16 v[144:147], a[56:59], a[236:239], v[144:147]\n"
    "v_pk_add_f32 v[216:217], v[216:217], v[84:85]\n"
    "v_pk_add_f32 v[218:219], v[218:219], v[86:87]\n"
    "v_mfma_f32_16x16x32_bf16 v[144:147], a[48:51], a[232:235], v[144:147]\n"
    "v_pk_add_f32 v[220:221], v[220:221], v[88:89]\n"
    "v_pk_add_f32 v[222:223], v[222:223], v[90:91]\n"
    "v_mfma_f32_16x16x32_bf16 v[144:147], a[40:43], a[228:231], v[144:147]\n"
    "v_pk_add_f32 v[224:225], v[224:225], v[84:85]\n"
    "v_pk_add_f32 v[226:227], v[226:227], v[86:87]\n"
    "v_mfma_f32_16x16x32_bf16 v[144:147], a[32:35], a[224:227], v[144:147]\n"
    "s_waitcnt vmcnt(20) lgkmcnt(0)\n"
    "s_barrier\n"
    "v_mfma_f32_16x16x32_bf16 v[152:155], a[32:35], a[240:243], v[152:155]\n"
    "s_add_u32 m0, s57, 65536\n"
    "ds_read_b128 a[208:211], v21 offset:0\n"
    "v_add_u32_dpp v17, v15, v2 row_newbcast:0 row_mask:0xf bank_mask:0xf\n"
    "v_mfma_f32_16x16x32_bf16 v[152:155], a[40:43], a[244:247], v[152:155]\n"
    "ds_read_b128 a[212:215], v22 offset:0\n"
    "v_pk_add_f32 v[228:229], v[228:229], v[88:89]\n"
    "v_mfma_f32_16x16x32_bf16 v[152:155], a[48:51], a[248:251], v[152:155]\n"
    "ds_read_b128 a[216:219], v23 offset:0\n"
    "buffer_load_dwordx4 v17, s[48:51], 0 offen offset:0 lds\n"
    "v_mfma_f32_16x16x32_bf16 v[152:155], a[56:59], a[252:255], v[152:155]\n"
    "buffer_store_dwordx4 v[136:139], v53, s[52:55], s35 offen offset:0 sc1\n"
    "v_mfma_f32_16x16x32_bf16 v[156:159], a[60:63], a[252:255], v[156:159]\n"
    "ds_read_b128 a[220:223], v24 offset:0\n"
    "v_add_u32_dpp v18, v15, v3 row_newbcast:8 row_mask:0xf bank_mask:0xf\n"
    "v_mfma_f32_16x16x32_bf16 v[156:159], a[52:55], a[248:251], v[156:159]\n"
    "ds_read_b128 a[224:227], v21 offset:1024\n"
    "v_cvt_pk_bf16_f32 v144, v144, v145\n"
    "v_mfma_f32_16x16x32_bf16 v[156:159], a[44:47], a[244:247], v[156:159]\n"
    "ds_read_b128 a[228:231], v22 offset:1024\n"
    "v_cvt_pk_bf16_f32 v145, v146, v147\n"
    "v_mfma_f32_16x16x32_bf16 v[156:159], a[36:39], a[240:243], v[156:159]\n"
    "ds_read_b128 a[232:235], v23 offset:1024\n"
    "v_cvt_pk_bf16_f32 v146, v148, v149\n"
    "v_mfma_f32_16x16x32_bf16 v[164:167], a[36:39], v[232:235], v[164:167]\n"
    "ds_read_b128 a[236:239], v24 offset:1024\n"
    "v_cvt_pk_bf16_f32 v147, v150, v151\n"
    "v_mfma_f32_16x16x32_bf16 v[164:167], a[44:47], v[236:239], v[164:167]\n"
    "ds_read_b128 a[240:243], v21 offset:40960\n"
    "v_pk_add_f32 v[230:231], v[230:231], v[90:91]\n"
    "v_mfma_f32_16x16x32_bf16 v[164:167], a[52:55], v[240:243], v[164:167]\n"
    "ds_read_b128 a[244:247], v22 offset:40960\n"
    "v_bfe_u32 v68, v1, 2, 2\n"
    "v_mfma_f32_16x16x32_bf16 v[164:167], a[60:63], v[244:247], v[164:167]\n"
    "ds_read_b128 a[248:251], v23 offset:40960\n"
    "v_xor_b32 v69, 0, v68\n"
    "v_mfma_f32_16x16x32_bf16 v[160:163], a[56:59], v[244:247], v[160:163]\n"
    "ds_read_b128 a[252:255], v24 offset:40960\n"
    "v_subrev_u32 v69, 1, v69\n"
    "v_mfma_f32_16x16x32_bf16 v[160:163], a[48:51], v[240:243], v[160:163]\n"
    "v_ashrrev_i32 v69, 31, v69\n"
    "v_and_b32 v104, v69, v80\n"
    "v_mfma_f32_16x16x32_bf16 v[160:163], a[40:43], v[236:239], v[160:163]\n"
    "v_and_b32 v105, v69, v81\n"
    "v_and_b32 v106, v69, v82\n"
    "v_mfma_f32_16x16x32_bf16 v[160:163], a[32:35], v[232:235], v[160:163]\n"
    "s_waitcnt lgkmcnt(4)\n"
    "ds_read_b128 v[232:235], v21 offset:41984\n"
    "buffer_load_dwordx4 v18, s[48:51], 0 offen offset:1024 lds\n"
    "v_mfma_f32_16x16x32_bf16 v[152:155], a[128:131], a[208:211], v[152:155]\n"
    "s_add_u32 m0, s57, 147456\n"
    "ds_read_b128 v[236:239], v22 offset:41984\n"
    "v_add_u32_dpp v19, v16, v2 row_newbcast:1 row_mask:0xf bank_mask:0xf\n"
    "v_mfma_f32_16x16x32_bf16 v[152:155], a[136:139], a[212:215], v[152:155]\n"
    "buffer_store_dwordx4 v[144:147], v53, s[52:55], s35 offen offset:1024 sc1\n"
    "v_mfma_f32_16x16x32_bf16 v[152:155], a[144:147], a[216:219], v[152:155]\n"
    "ds_read_b128 v[240:243], v23 offset:41984\n"
    "v_and_b32 v107, v69, v83\n"
    "v_mfma_f32_16x16x32_bf16 v[152:155], a[152:155], a[220:223], v[152:155]\n"
    "ds_read_b128 v[244:247], v24 offset:41984\n"
    "v_xor_b32 v69, 1, v68\n"
    "v_mfma_f32_16x16x32_bf16 v[156:159], a[156:159], a[220:223], v[156:159]\n"
    "v_subrev_u32 v69, 1, v69\n"
    "v_ashrrev_i32 v69, 31, v69\n"
    "v_mfma_f32_16x16x32_bf16 v[156:159], a[148:151], a[216:219], v[156:159]\n"
    "v_and_b32 v112, v69, v80\n"
    "v_and_b32 v113, v69, v81\n"
    "v_mfma_f32_16x16x32_bf16 v[156:159], a[140:143], a[212:215], v[156:159]\n"
    "v_and_b32 v114, v69, v82\n"
    "v_and_b32 v115, v69, v83\n"
    "v_mfma_f32_16x16x32_bf16 v[156:159], a[132:135], a[208:211], v[156:159]\n"
    "v_xor_b32 v69, 2, v68\n"
    "v_subrev_u32 v69, 1, v69\n"
    "v_mfma_f32_16x16x32_bf16 v[164:167], a[132:135], a[224:227], v[164:167]\n"
    "v_ashrrev_i32 v69, 31, v69\n"
    "v_and_b32 v120, v69, v80\n"
    "v_mfma_f32_16x16x32_bf16 v[164:167], a[140:143], a[228:231], v[164:167]\n"
    "v_and_b32 v121, v69, v81\n"
    "v_and_b32 v122, v69, v82\n"
    "v_mfma_f32_16x16x32_bf16 v[164:167], a[148:151], a[232:235], v[164:167]\n"
    "v_and_b32 v123, v69, v83\n"
    "v_xor_b32 v69, 3, v68\n"
    "v_mfma_f32_16x16x32_bf16 v[164:167], a[156:159], a[236:239], v[164:167]\n"
    "v_subrev_u32 v69, 1, v69\n"
    "v_ashrrev_i32 v69, 31, v69\n"
    "v_mfma_f32_16x16x32_bf16 v[160:163], a[152:155], a[236:239], v[160:163]\n"
    "v_and_b32 v128, v69, v80\n"
    "v_and_b32 v129, v69, v81\n"
    "v_mfma_f32_16x16x32_bf16 v[160:163], a[144:147], a[232:235], v[160:163]\n"
    "v_and_b32 v130, v69, v82\n"
    "v_and_b32 v131, v69, v83\n"
    "v_mfma_f32_16x16x32_bf16 v[160:163], a[136:139], a[228:231], v[160:163]\n"
    "v_mfma_f32_16x16x32_bf16 v[160:163], a[128:131], a[224:227], v[160:163]\n"
    "s_waitcnt vmcnt(19) lgkmcnt(0)\n"
    "s_barrier\n"
    "v_mfma_f32_16x16x32_bf16 v[152:155], a[160:163], a[240:243], v[152:155]\n"
    "ds_read_b128 a[208:211], v21 offset:24576\n"
    "buffer_load_dwordx4 v19, s[48:51], 0 offen offset:0 lds\n"
    "v_mfma_f32_16x16x32_bf16 v[152:155], a[168:171], a[244:247], v[152:155]\n"
    "ds_read_b128 a[212:215], v22 offset:24576\n"
    "v_add_u32_dpp v20, v16, v3 row_newbcast:9 row_mask:0xf bank_mask:0xf\n"
    "v_mfma_f32_16x16x32_bf16 v[152:155], a[176:179], a[248:251], v[152:155]\n"
    "ds_read_b128 a[216:219], v23 offset:24576\n"
    "buffer_load_dwordx4 v20, s[48:51], 0 offen offset:1024 lds\n"
    "v_mfma_f32_16x16x32_bf16 v[152:155], a[184:187], a[252:255], v[152:155]\n"
    "ds_read_b128 a[220:223], v24 offset:24576\n"
    "v_mfma_f32_16x16x32_bf16 v[156:159], a[188:191], a[252:255], v[156:159]\n"
    "ds_read_b128 a[224:227], v21 offset:25600\n"
    "v_mfma_f32_16x16x32_bf16 v[156:159], a[180:183], a[248:251], v[156:159]\n"
    "ds_read_b128 a[228:231], v22 offset:25600\n"
    "v_mfma_f32_16x16x32_bf16 v[156:159], a[172:175], a[244:247], v[156:159]\n"
    "ds_read_b128 a[232:235], v23 offset:25600\n"
    "v_mfma_f32_16x16x32_bf16 v[156:159], a[164:167], a[240:243], v[156:159]\n"
    "ds_read_b128 a[236:239], v24 offset:25600\n"
    "v_mfma_f32_16x16x32_bf16 v[164:167], a[164:167], v[232:235], v[164:167]\n"
    "ds_read_b128 a[240:243], v21 offset:8192\n"
    "v_mfma_f32_16x16x32_bf16 v[164:167], a[172:175], v[236:239], v[164:167]\n"
    "ds_read_b128 a[244:247], v22 offset:8192\n"
    "v_mfma_f32_16x16x32_bf16 v[164:167], a[180:183], v[240:243], v[164:167]\n"
    "ds_read_b128 a[248:251], v23 offset:8192\n"
    "v_mfma_f32_16x16x32_bf16 v[164:167], a[188:191], v[244:247], v[164:167]\n"
    "ds_read_b128 a[252:255], v24 offset:8192\n"
    "v_cvt_pk_bf16_f32 v152, v152, v153\n"
    "v_mfma_f32_16x16x32_bf16 v[160:163], a[184:187], v[244:247], v[160:163]\n"
    "v_cvt_pk_bf16_f32 v153, v154, v155\n"
    "v_cvt_pk_bf16_f32 v154, v156, v157\n"
    "v_mfma_f32_16x16x32_bf16 v[160:163], a[176:179], v[240:243], v[160:163]\n"
    "v_cvt_pk_bf16_f32 v155, v158, v159\n"
    "v_mfma_f32_16x16x32_bf16 v[160:163], a[168:171], v[236:239], v[160:163]\n"
    "buffer_store_dwordx4 v[152:155], v53, s[52:55], s35 offen offset:2048 sc1\n"
    "v_mfma_f32_16x16x32_bf16 v[160:163], a[160:163], v[232:235], v[160:163]\n"
    "s_waitcnt lgkmcnt(3)\n"
    "ds_read_b128 v[232:235], v21 offset:9216\n"
    "v_mfma_f32_16x16x32_bf16 v[168:171], a[32:35], a[208:211], v[168:171]\n"
    "ds_read_b128 v[236:239], v22 offset:9216\n"
    "v_mfma_f32_16x16x32_bf16 v[168:171], a[40:43], a[212:215], v[168:171]\n"
    "ds_read_b128 v[240:243], v23 offset:9216\n"
    "v_cmp_ne_u32 vcc, 1, v56\n"
    "v_mfma_f32_16x16x32_bf16 v[168:171], a[48:51], a[216:219], v[168:171]\n"
    "s_cbranch_vccnz .Lwr_b%=\n"
    ".Lokr_b%=:\n"
    "ds_read_b128 v[244:247], v24 offset:9216\n"
    "buffer_store_dword v57, v54, s[52:55], 0 offen sc1\n"
    "v_mfma_f32_16x16x32_bf16 v[168:171], a[56:59], a[220:223], v[168:171]\n"
    "s_waitcnt vmcnt(8)\n"
    "s_xor_b32 s62, s59, 256\n"
    "v_cvt_pk_bf16_f32 v160, v160, v161\n"
    "v_mfma_f32_16x16x32_bf16 v[172:175], a[60:63], a[220:223], v[172:175]\n"
    "buffer_store_dword v57, off, s[52:55], s62 offset:192 sc1\n"
    "v_cvt_pk_bf16_f32 v161, v162, v163\n"
    "v_mfma_f32_16x16x32_bf16 v[172:175], a[52:55], a[216:219], v[172:175]\n"
    "v_cvt_pk_bf16_f32 v162, v164, v165\n"
    "v_cvt_pk_bf16_f32 v163, v166, v167\n"
    "v_mfma_f32_16x16x32_bf16 v[172:175], a[44:47], a[212:215], v[172:175]\n"
    "v_mfma_f32_16x16x32_bf16 v[172:175], a[36:39], a[208:211], v[172:175]\n"
    "v_mfma_f32_16x16x32_bf16 v[180:183], a[36:39], a[224:227], v[180:183]\n"
    "v_mfma_f32_16x16x32_bf16 v[180:183], a[44:47], a[228:231], v[180:183]\n"
    "v_mfma_f32_16x16x32_bf16 v[180:183], a[52:55], a[232:235], v[180:183]\n"
    "v_mfma_f32_16x16x32_bf16 v[180:183], a[60:63], a[236:239], v[180:183]\n"
    "v_mfma_f32_16x16x32_bf16 v[176:179], a[56:59], a[236:239], v[176:179]\n"
    "buffer_store_dwordx4 v[160:163], v53, s[52:55], s35 offen offset:3072 sc1\n"
    "v_mfma_f32_16x16x32_bf16 v[176:179], a[48:51], a[232:235], v[176:179]\n"
    "v_mfma_f32_16x16x32_bf16 v[176:179], a[40:43], a[228:231], v[176:179]\n"
    "v_cvt_pk_bf16_f32 v168, v168, v169\n"
    "v_cvt_pk_bf16_f32 v169, v170, v171\n"
    "v_mfma_f32_16x16x32_bf16 v[176:179], a[32:35], a[224:227], v[176:179]\n"
    "s_waitcnt lgkmcnt(0)\n"
    "s_barrier\n"
    "v_mfma_f32_16x16x32_bf16 v[184:187], a[32:35], a[240:243], v[184:187]\n"
    "ds_read_b128 a[208:211], v21 offset:16384\n"
    "v_cvt_pk_bf16_f32 v170, v172, v173\n"
    "v_mfma_f32_16x16x32_bf16 v[184:187], a[40:43], a[244:247], v[184:187]\n"
    "ds_read_b128 a[212:215], v22 offset:16384\n"
    "v_cvt_pk_bf16_f32 v171, v174, v175\n"
    "v_mfma_f32_16x16x32_bf16 v[184:187], a[48:51], a[248:251], v[184:187]\n"
    "ds_read_b128 a[216:219], v23 offset:16384\n"
    "v_mfma_f32_16x16x32_bf16 v[184:187], a[56:59], a[252:255], v[184:187]\n"
    "ds_read_b128 a[220:223], v24 offset:16384\n"
    "v_mfma_f32_16x16x32_bf16 v[188:191], a[60:63], a[252:255], v[188:191]\n"
    "ds_read_b128 a[224:227], v21 offset:17408\n"
    "v_mfma_f32_16x16x32_bf16 v[188:191], a[52:55], a[248:251], v[188:191]\n"
    "ds_read_b128 a[228:231], v22 offset:17408\n"
    "v_mfma_f32_16x16x32_bf16 v[188:191], a[44:47], a[244:247], v[188:191]\n"
    "ds_read_b128 a[232:235], v23 offset:17408\n"
    "v_mfma_f32_16x16x32_bf16 v[188:191], a[36:39], a[240:243], v[188:191]\n"
    "ds_read_b128 a[236:239], v24 offset:17408\n"
    "v_mfma_f32_16x16x32_bf16 v[196:199], a[36:39], v[232:235], v[196:199]\n"
    "ds_read_b128 a[240:243], v21 offset:49152\n"
    "v_mfma_f32_16x16x32_bf16 v[196:199], a[44:47], v[236:239], v[196:199]\n"
    "ds_read_b128 a[244:247], v22 offset:49152\n"
    "v_mfma_f32_16x16x32_bf16 v[196:199], a[52:55], v[240:243], v[196:199]\n"
    "buffer_store_dwordx4 v[168:171], v53, s[52:55], s36 offen offset:0 sc1\n"
    "v_mfma_f32_16x16x32_bf16 v[196:199], a[60:63], v[244:247], v[196:199]\n"
    "ds_read_b128 a[248:251], v23 offset:49152\n"
    "v_cvt_pk_bf16_f32 v176, v176, v177\n"
    "v_mfma_f32_16x16x32_bf16 v[192:195], a[56:59], v[244:247], v[192:195]\n"
    "ds_read_b128 a[252:255], v24 offset:49152\n"
    "v_cvt_pk_bf16_f32 v177, v178, v179\n"
    "v_mfma_f32_16x16x32_bf16 v[192:195], a[48:51], v[240:243], v[192:195]\n"
    "v_cvt_pk_bf16_f32 v178, v180, v181\n"
    "v_cvt_pk_bf16_f32 v179, v182, v183\n"
    "v_mfma_f32_16x16x32_bf16 v[192:195], a[40:43], v[236:239], v[192:195]\n"
    "v_mfma_f32_16x16x32_bf16 v[192:195], a[32:35], v[232:235], v[192:195]\n"
    "s_waitcnt lgkmcnt(3)\n"
    "ds_read_b128 v[232:235], v21 offset:50176\n"
    "v_mfma_f32_16x16x32_bf16 v[184:187], a[128:131], a[208:211], v[184:187]\n"
    "ds_read_b128 v[236:239], v22 offset:50176\n"
    "v_mfma_f32_16x16x32_bf16 v[184:187], a[136:139], a[212:215], v[184:187]\n"
    "ds_read_b128 v[240:243], v23 offset:50176\n"
    "v_mfma_f32_16x16x32_bf16 v[184:187], a[144:147], a[216:219], v[184:187]\n"
    "ds_read_b128 v[244:247], v24 offset:50176\n"
    "v_mfma_f32_16x16x32_bf16 v[184:187], a[152:155], a[220:223], v[184:187]\n"
    "v_mfma_f32_16x16x32_bf16 v[188:191], a[156:159], a[220:223], v[188:191]\n"
    "v_mfma_f32_16x16x32_bf16 v[188:191], a[148:151], a[216:219], v[188:191]\n"
    "v_mfma_f32_16x16x32_bf16 v[188:191], a[140:143], a[212:215], v[188:191]\n"
    "v_mfma_f32_16x16x32_bf16 v[188:191], a[132:135], a[208:211], v[188:191]\n"
    "v_mfma_f32_16x16x32_bf16 v[196:199], a[132:135], a[224:227], v[196:199]\n"
    "buffer_store_dwordx4 v[176:179], v53, s[52:55], s36 offen offset:1024 sc1\n"
    "v_mfma_f32_16x16x32_bf16 v[196:199], a[140:143], a[228:231], v[196:199]\n"
    "v_mfma_f32_16x16x32_bf16 v[196:199], a[148:151], a[232:235], v[196:199]\n"
    "v_mfma_f32_16x16x32_bf16 v[196:199], a[156:159], a[236:239], v[196:199]\n"
    "v_mfma_f32_16x16x32_bf16 v[192:195], a[152:155], a[236:239], v[192:195]\n"
    "v_mfma_f32_16x16x32_bf16 v[192:195], a[144:147], a[232:235], v[192:195]\n"
    "v_mfma_f32_16x16x32_bf16 v[192:195], a[136:139], a[228:231], v[192:195]\n"
    "v_mfma_f32_16x16x32_bf16 v[192:195], a[128:131], a[224:227], v[192:195]\n"
    "s_waitcnt lgkmcnt(0)\n"
    "s_barrier\n"
    "v_mfma_f32_16x16x32_bf16 v[184:187], a[160:163], a[240:243], v[184:187]\n"
    "ds_read_b128 a[208:211], v21 offset:32768\n"
    "v_mfma_f32_16x16x32_bf16 v[184:187], a[168:171], a[244:247], v[184:187]\n"
    "ds_read_b128 a[212:215], v22 offset:32768\n"
    "v_mfma_f32_16x16x32_bf16 v[184:187], a[176:179], a[248:251], v[184:187]\n"
    "ds_read_b128 a[216:219], v23 offset:32768\n"
    "v_mfma_f32_16x16x32_bf16 v[184:187], a[184:187], a[252:255], v[184:187]\n"
    "ds_read_b128 a[220:223], v24 offset:32768\n"
    "v_mfma_f32_16x16x32_bf16 v[188:191], a[188:191], a[252:255], v[188:191]\n"
    "ds_read_b128 a[224:227], v21 offset:33792\n"
    "v_mfma_f32_16x16x32_bf16 v[188:191], a[180:183], a[248:251], v[188:191]\n"
    "ds_read_b128 a[228:231], v22 offset:33792\n"
    "v_mfma_f32_16x16x32_bf16 v[188:191], a[172:175], a[244:247], v[188:191]\n"
    "ds_read_b128 a[232:235], v23 offset:33792\n"
    "v_mfma_f32_16x16x32_bf16 v[188:191], a[164:167], a[240:243], v[188:191]\n"
    "ds_read_b128 a[236:239], v24 offset:33792\n"
    "v_mfma_f32_16x16x32_bf16 v[196:199], a[164:167], v[232:235], v[196:199]\n"
    "ds_read_b128 a[240:243], v21 offset:57344\n"
    "v_mfma_f32_16x16x32_bf16 v[196:199], a[172:175], v[236:239], v[196:199]\n"
    "ds_read_b128 a[244:247], v22 offset:57344\n"
    "v_mfma_f32_16x16x32_bf16 v[196:199], a[180:183], v[240:243], v[196:199]\n"
    "ds_read_b128 a[248:251], v23 offset:57344\n"
    "v_mfma_f32_16x16x32_bf16 v[196:199], a[188:191], v[244:247], v[196:199]\n"
    "ds_read_b128 a[252:255], v24 offset:57344\n"
    "v_cvt_pk_bf16_f32 v184, v184, v185\n"
    "v_mfma_f32_16x16x32_bf16 v[192:195], a[184:187], v[244:247], v[192:195]\n"
    "v_cvt_pk_bf16_f32 v185, v186, v187\n"
    "v_cvt_pk_bf16_f32 v186, v188, v189\n"
    "v_mfma_f32_16x16x32_bf16 v[192:195], a[176:179], v[240:243], v[192:195]\n"
    "v_cvt_pk_bf16_f32 v187, v190, v191\n"
    "v_mfma_f32_16x16x32_bf16 v[192:195], a[168:171], v[236:239], v[192:195]\n"
    "buffer_store_dwordx4 v[184:187], v53, s[52:55], s36 offen offset:2048 sc1\n"
    "v_mfma_f32_16x16x32_bf16 v[192:195], a[160:163], v[232:235], v[192:195]\n"
    "s_waitcnt lgkmcnt(3)\n"
    "ds_read_b128 v[232:235], v21 offset:58368\n"
    "v_mfma_f32_16x16x32_bf16 v[200:203], a[64:67], a[208:211], v[200:203]\n"
    "ds_read_b128 v[236:239], v22 offset:58368\n"
    "v_mfma_f32_16x16x32_bf16 v[200:203], a[72:75], a[212:215], v[200:203]\n"
    "ds_read_b128 v[240:243], v23 offset:58368\n"
    "v_mfma_f32_16x16x32_bf16 v[200:203], a[80:83], a[216:219], v[200:203]\n"
    "ds_read_b128 v[244:247], v24 offset:58368\n"
    "v_mfma_f32_16x16x32_bf16 v[200:203], a[88:91], a[220:223], v[200:203]\n"
    "v_cvt_pk_bf16_f32 v192, v192, v193\n"
    "v_cvt_pk_bf16_f32 v193, v194, v195\n"
    "v_mfma_f32_16x16x32_bf16 v[204:207], a[92:95], a[220:223], v[204:207]\n"
    "v_cvt_pk_bf16_f32 v194, v196, v197\n"
    "v_cvt_pk_bf16_f32 v195, v198, v199\n"
    "v_mfma_f32_16x16x32_bf16 v[204:207], a[84:87], a[216:219], v[204:207]\n"
    "s_add_u32 m0, s57, 81920\n"
    "v_add_u32_dpp v17, v16, v2 row_newbcast:0 row_mask:0xf bank_mask:0xf\n"
    "v_mfma_f32_16x16x32_bf16 v[204:207], a[76:79], a[212:215], v[204:207]\n"
    "v_mfma_f32_16x16x32_bf16 v[204:207], a[68:71], a[208:211], v[204:207]\n"
    "v_mfma_f32_16x16x32_bf16 v[212:215], a[68:71], a[224:227], v[212:215]\n"
    "v_mfma_f32_16x16x32_bf16 v[212:215], a[76:79], a[228:231], v[212:215]\n"
    "v_mfma_f32_16x16x32_bf16 v[212:215], a[84:87], a[232:235], v[212:215]\n"
    "buffer_load_dwordx4 v17, s[48:51], 0 offen offset:0 lds\n"
    "v_add_u32_dpp v18, v16, v3 row_newbcast:8 row_mask:0xf bank_mask:0xf\n"
    "v_mfma_f32_16x16x32_bf16 v[212:215], a[92:95], a[236:239], v[212:215]\n"
    "buffer_load_dwordx4 v18, s[48:51], 0 offen offset:1024 lds\n"
    "s_add_u32 m0, s57, 98304\n"
    "v_mfma_f32_16x16x32_bf16 v[208:211], a[88:91], a[236:239], v[208:211]\n"
    "buffer_store_dwordx4 v[192:195], v53, s[52:55], s36 offen offset:3072 sc1\n"
    "v_mfma_f32_16x16x32_bf16 v[208:211], a[80:83], a[232:235], v[208:211]\n"
    "v_add_u32_dpp v19, v15, v2 row_newbcast:1 row_mask:0xf bank_mask:0xf\n"
    "v_mfma_f32_16x16x32_bf16 v[208:211], a[72:75], a[228:231], v[208:211]\n"
    "v_mfma_f32_16x16x32_bf16 v[208:211], a[64:67], a[224:227], v[208:211]\n"
    "s_waitcnt lgkmcnt(0)\n"
    "s_barrier\n"
    "v_mfma_f32_16x16x32_bf16 v[216:219], a[96:99], a[240:243], v[216:219]\n"
    "ds_read_b128 a[208:211], v25 offset:24576\n"
    "v_mfma_f32_16x16x32_bf16 v[216:219], a[104:107], a[244:247], v[216:219]\n"
    "ds_read_b128 a[212:215], v26 offset:24576\n"
    "buffer_load_dword v59, off, s[52:55], s59 offset:192 sc1\n"
    "v_mfma_f32_16x16x32_bf16 v[216:219], a[112:115], a[248:251], v[216:219]\n"
    "s_waitcnt vmcnt(7)\n"
    "ds_read_b128 a[216:219], v27 offset:24576\n"
    "buffer_load_dwordx4 v19, s[48:51], 0 offen offset:0 lds\n"
    "v_mfma_f32_16x16x32_bf16 v[216:219], a[120:123], a[252:255], v[216:219]\n"
    "s_xor_b32 s62, s59, 512\n"
    "ds_read_b128 a[220:223], v28 offset:24576\n"
    "v_add_u32_dpp v20, v15, v3 row_newbcast:9 row_mask:0xf bank_mask:0xf\n"
    "v_mfma_f32_16x16x32_bf16 v[220:223], a[124:127], a[252:255], v[220:223]\n"
    "ds_read_b128 a[224:227], v25 offset:25600\n"
    "buffer_store_dword v57, off, s[52:55], s62 offset:208 sc1\n"
    "v_mfma_f32_16x16x32_bf16 v[220:223], a[116:119], a[248:251], v[220:223]\n"
    "ds_read_b128 a[228:231], v26 offset:25600\n"
    "buffer_load_dwordx4 v20, s[48:51], 0 offen offset:1024 lds\n"
    "v_mfma_f32_16x16x32_bf16 v[220:223], a[108:111], a[244:247], v[220:223]\n"
    "s_add_u32 m0, s57, 106496\n"
    "ds_read_b128 a[232:235], v27 offset:25600\n"
    "v_add_u32_dpp v17, v15, v2 row_newbcast:5 row_mask:0xf bank_mask:0xf\n"
    "v_mfma_f32_16x16x32_bf16 v[220:223], a[100:103], a[240:243], v[220:223]\n"
    "ds_read_b128 a[236:239], v28 offset:25600\n"
    "buffer_load_dwordx4 v17, s[48:51], 0 offen offset:0 lds\n"
    "v_mfma_f32_16x16x32_bf16 v[228:231], a[100:103], v[232:235], v[228:231]\n"
    "ds_read_b128 a[240:243], v25 offset:8192\n"
    "v_add_u32_dpp v18, v15, v3 row_newbcast:13 row_mask:0xf bank_mask:0xf\n"
    "v_mfma_f32_16x16x32_bf16 v[228:231], a[108:111], v[236:239], v[228:231]\n"
    "ds_read_b128 a[244:247], v26 offset:8192\n"
    "buffer_load_dwordx4 v18, s[48:51], 0 offen offset:1024 lds\n"
    "v_mfma_f32_16x16x32_bf16 v[228:231], a[116:119], v[240:243], v[228:231]\n"
    "s_add_u32 m0, s57, 155648\n"
    "ds_read_b128 a[248:251], v27 offset:8192\n"
    "v_add_u32_dpp v19, v16, v2 row_newbcast:5 row_mask:0xf bank_mask:0xf\n"
    "v_mfma_f32_16x16x32_bf16 v[228:231], a[124:127], v[244:247], v[228:231]\n"
    "ds_read_b128 a[252:255], v28 offset:8192\n"
    "buffer_load_dwordx4 v19, s[48:51], 0 offen offset:0 lds\n"
    "v_mfma_f32_16x16x32_bf16 v[224:227], a[120:123], v[244:247], v[224:227]\n"
    "v_add_u32_dpp v20, v16, v3 row_newbcast:13 row_mask:0xf bank_mask:0xf\n"
    "v_mfma_f32_16x16x32_bf16 v[224:227], a[112:115], v[240:243], v[224:227]\n"
    "buffer_load_dwordx4 v20, s[48:51], 0 offen offset:1024 lds\n"
    "s_add_u32 m0, s57, 114688\n"
    "v_mfma_f32_16x16x32_bf16 v[224:227], a[104:107], v[236:239], v[224:227]\n"
    "v_add_u32_dpp v17, v15, v2 row_newbcast:4 row_mask:0xf bank_mask:0xf\n"
    "v_mfma_f32_16x16x32_bf16 v[224:227], a[96:99], v[232:235], v[224:227]\n"
    "s_waitcnt lgkmcnt(3)\n"
    "ds_read_b128 v[232:235], v25 offset:9216\n"
    "v_mfma_f32_16x16x32_bf16 v[216:219], a[64:67], a[208:211], v[216:219]\n"
    "ds_read_b128 v[236:239], v26 offset:9216\n"
    "v_mfma_f32_16x16x32_bf16 v[216:219], a[72:75], a[212:215], v[216:219]\n"
    "ds_read_b128 v[240:243], v27 offset:9216\n"
    "v_mfma_f32_16x16x32_bf16 v[216:219], a[80:83], a[216:219], v[216:219]\n"
    "ds_read_b128 v[244:247], v28 offset:9216\n"
    "buffer_load_dwordx4 v17, s[48:51], 0 offen offset:0 lds\n"
    "v_mfma_f32_16x16x32_bf16 v[216:219], a[88:91], a[220:223], v[216:219]\n"
    "v_add_u32_dpp v18, v15, v3 row_newbcast:12 row_mask:0xf bank_mask:0xf\n"
    "v_mfma_f32_16x16x32_bf16 v[220:223], a[92:95], a[220:223], v[220:223]\n"
    "v_mfma_f32_16x16x32_bf16 v[220:223], a[84:87], a[216:219], v[220:223]\n"
    "v_mfma_f32_16x16x32_bf16 v[220:223], a[76:79], a[212:215], v[220:223]\n"
    "v_mfma_f32_16x16x32_bf16 v[220:223], a[68:71], a[208:211], v[220:223]\n"
    "v_mfma_f32_16x16x32_bf16 v[228:231], a[68:71], a[224:227], v[228:231]\n"
    "v_mfma_f32_16x16x32_bf16 v[228:231], a[76:79], a[228:231], v[228:231]\n"
    "v_mfma_f32_16x16x32_bf16 v[228:231], a[84:87], a[232:235], v[228:231]\n"
    "v_mfma_f32_16x16x32_bf16 v[228:231], a[92:95], a[236:239], v[228:231]\n"
    "v_mfma_f32_16x16x32_bf16 v[224:227], a[88:91], a[236:239], v[224:227]\n"
    "v_mfma_f32_16x16x32_bf16 v[224:227], a[80:83], a[232:235], v[224:227]\n"
    "v_mfma_f32_16x16x32_bf16 v[224:227], a[72:75], a[228:231], v[224:227]\n"
    "v_mfma_f32_16x16x32_bf16 v[224:227], a[64:67], a[224:227], v[224:227]\n"
    "s_waitcnt lgkmcnt(0)\n"
    "s_barrier\n"
    "v_mfma_f32_16x16x32_bf16 v[200:203], a[96:99], a[240:243], v[200:203]\n"
    "ds_read_b128 a[208:211], v25 offset:0\n"
    "v_mfma_f32_16x16x32_bf16 v[200:203], a[104:107], a[244:247], v[200:203]\n"
    "ds_read_b128 a[212:215], v26 offset:0\n"
    "v_mfma_f32_16x16x32_bf16 v[200:203], a[112:115], a[248:251], v[200:203]\n"
    "ds_read_b128 a[216:219], v27 offset:0\n"
    "v_mfma_f32_16x16x32_bf16 v[200:203], a[120:123], a[252:255], v[200:203]\n"
    "ds_read_b128 a[220:223], v28 offset:0\n"
    "v_mfma_f32_16x16x32_bf16 v[204:207], a[124:127], a[252:255], v[204:207]\n"
    "ds_read_b128 a[224:227], v25 offset:1024\n"
    "v_mfma_f32_16x16x32_bf16 v[204:207], a[116:119], a[248:251], v[204:207]\n"
    "ds_read_b128 a[228:231], v26 offset:1024\n"
    "v_mfma_f32_16x16x32_bf16 v[204:207], a[108:111], a[244:247], v[204:207]\n"
    "ds_read_b128 a[232:235], v27 offset:1024\n"
    "v_mfma_f32_16x16x32_bf16 v[204:207], a[100:103], a[240:243], v[204:207]\n"
    "ds_read_b128 a[236:239], v28 offset:1024\n"
    "v_mfma_f32_16x16x32_bf16 v[212:215], a[100:103], v[232:235], v[212:215]\n"
    "ds_read_b128 a[240:243], v29 offset:16384\n"
    "v_mfma_f32_16x16x32_bf16 v[212:215], a[108:111], v[236:239], v[212:215]\n"
    "ds_read_b128 a[244:247], v30 offset:16384\n"
    "v_mfma_f32_16x16x32_bf16 v[212:215], a[116:119], v[240:243], v[212:215]\n"
    "ds_read_b128 a[248:251], v31 offset:16384\n"
    "v_mfma_f32_16x16x32_bf16 v[212:215], a[124:127], v[244:247], v[212:215]\n"
    "ds_read_b128 a[252:255], v32 offset:16384\n"
    "v_mfma_f32_16x16x32_bf16 v[208:211], a[120:123], v[244:247], v[208:211]\n"
    "v_mfma_f32_16x16x32_bf16 v[208:211], a[112:115], v[240:243], v[208:211]\n"
    "v_mfma_f32_16x16x32_bf16 v[208:211], a[104:107], v[236:239], v[208:211]\n"
    "s_waitcnt vmcnt(9)\n"
    "s_xor_b32 s62, s59, 768\n"
    "v_mfma_f32_16x16x32_bf16 v[208:211], a[96:99], v[232:235], v[208:211]\n"
    "s_waitcnt lgkmcnt(3)\n"
    "ds_read_b128 v[232:235], v29 offset:17408\n"
    "buffer_store_dword v57, off, s[52:55], s62 offset:224 sc1\n"
    "v_mfma_f32_16x16x32_bf16 v[200:203], a[0:3], a[208:211], v[200:203]\n"
    "ds_read_b128 v[236:239], v30 offset:17408\n"
    "v_mfma_f32_16x16x32_bf16 v[200:203], a[8:11], a[212:215], v[200:203]\n"
    "ds_read_b128 v[240:243], v31 offset:17408\n"
    "v_mfma_f32_16x16x32_bf16 v[200:203], a[16:19], a[216:219], v[200:203]\n"
    "ds_read_b128 v[244:247], v32 offset:17408\n"
    "v_mfma_f32_16x16x32_bf16 v[200:203], a[24:27], a[220:223], v[200:203]\n"
    "s_waitcnt vmcnt(9)\n"
    "buffer_load_dwordx4 v18, s[48:51], 0 offen offset:1024 lds\n"
    "v_cmp_ne_u32 vcc, 1, v59\n"
    "v_mfma_f32_16x16x32_bf16 v[204:207], a[28:31], a[220:223], v[204:207]\n"
    "s_cbranch_vccnz .Lwf0_b%=\n"
    ".Lokf0_b%=:\n"
    "s_add_u32 s62, s98, 0\n"
    "s_add_u32 m0, s57, 122880\n"
    "v_mfma_f32_16x16x32_bf16 v[204:207], a[20:23], a[216:219], v[204:207]\n"
    "buffer_load_dwordx4 v[108:111], v53, s[52:55], s62 offen offset:0 sc1\n"
    "v_add_u32_dpp v19, v16, v2 row_newbcast:4 row_mask:0xf bank_mask:0xf\n"
    "v_mfma_f32_16x16x32_bf16 v[204:207], a[12:15], a[212:215], v[204:207]\n"
    "buffer_load_dwordx4 v[116:119], v53, s[52:55], s62 offen offset:1024 sc1\n"
    "v_mfma_f32_16x16x32_bf16 v[204:207], a[4:7], a[208:211], v[204:207]\n"
    "buffer_load_dwordx4 v[124:127], v53, s[52:55], s62 offen offset:2048 sc1\n"
    "v_mfma_f32_16x16x32_bf16 v[212:215], a[4:7], a[224:227], v[212:215]\n"
    "buffer_load_dwordx4 v[132:135], v53, s[52:55], s62 offen offset:3072 sc1\n"
    "v_mfma_f32_16x16x32_bf16 v[212:215], a[12:15], a[228:231], v[212:215]\n"
    "v_mfma_f32_16x16x32_bf16 v[212:215], a[20:23], a[232:235], v[212:215]\n"
    "v_mfma_f32_16x16x32_bf16 v[212:215], a[28:31], a[236:239], v[212:215]\n"
    "v_mfma_f32_16x16x32_bf16 v[208:211], a[24:27], a[236:239], v[208:211]\n"
    "v_mfma_f32_16x16x32_bf16 v[208:211], a[16:19], a[232:235], v[208:211]\n"
    "buffer_load_dwordx4 v19, s[48:51], 0 offen offset:0 lds\n"
    "v_add_u32_dpp v20, v16, v3 row_newbcast:12 row_mask:0xf bank_mask:0xf\n"
    "v_mfma_f32_16x16x32_bf16 v[208:211], a[8:11], a[228:231], v[208:211]\n"
    "v_mfma_f32_16x16x32_bf16 v[208:211], a[0:3], a[224:227], v[208:211]\n"
    "s_waitcnt vmcnt(12) lgkmcnt(0)\n"
    "s_barrier\n"
    "v_mfma_f32_16x16x32_bf16 v[216:219], a[32:35], a[240:243], v[216:219]\n"
    "ds_read_b128 a[208:211], v25 offset:16384\n"
    "buffer_load_dwordx4 v20, s[48:51], 0 offen offset:1024 lds\n"
    "v_mfma_f32_16x16x32_bf16 v[216:219], a[40:43], a[244:247], v[216:219]\n"
    "ds_read_b128 a[212:215], v26 offset:16384\n"
    "v_mfma_f32_16x16x32_bf16 v[216:219], a[48:51], a[248:251], v[216:219]\n"
    "ds_read_b128 a[216:219], v27 offset:16384\n"
    "v_mfma_f32_16x16x32_bf16 v[216:219], a[56:59], a[252:255], v[216:219]\n"
    "ds_read_b128 a[220:223], v28 offset:16384\n"
    "v_mfma_f32_16x16x32_bf16 v[220:223], a[60:63], a[252:255], v[220:223]\n"
    "ds_read_b128 a[224:227], v25 offset:17408\n"
    "v_mfma_f32_16x16x32_bf16 v[220:223], a[52:55], a[248:251], v[220:223]\n"
    "ds_read_b128 a[228:231], v26 offset:17408\n"
    "v_mfma_f32_16x16x32_bf16 v[220:223], a[44:47], a[244:247], v[220:223]\n"
    "ds_read_b128 a[232:235], v27 offset:17408\n"
    "v_mfma_f32_16x16x32_bf16 v[220:223], a[36:39], a[240:243], v[220:223]\n"
    "ds_read_b128 a[236:239], v28 offset:17408\n"
    "v_mfma_f32_16x16x32_bf16 v[228:231], a[36:39], v[232:235], v[228:231]\n"
    "ds_read_b128 a[240:243], v25 offset:32768\n"
    "buffer_load_dword v60, off, s[52:55], s59 offset:208 sc1\n"
    "v_mfma_f32_16x16x32_bf16 v[228:231], a[44:47], v[236:239], v[228:231]\n"
    "ds_read_b128 a[244:247], v26 offset:32768\n"
    "v_mfma_f32_16x16x32_bf16 v[228:231], a[52:55], v[240:243], v[228:231]\n"
    "ds_read_b128 a[248:251], v27 offset:32768\n"
    "v_mfma_f32_16x16x32_bf16 v[228:231], a[60:63], v[244:247], v[228:231]\n"
    "ds_read_b128 a[252:255], v28 offset:32768\n"
    "v_mfma_f32_16x16x32_bf16 v[224:227], a[56:59], v[244:247], v[224:227]\n"
    "v_mfma_f32_16x16x32_bf16 v[224:227], a[48:51], v[240:243], v[224:227]\n"
    "v_mfma_f32_16x16x32_bf16 v[224:227], a[40:43], v[236:239], v[224:227]\n"
    "v_mfma_f32_16x16x32_bf16 v[224:227], a[32:35], v[232:235], v[224:227]\n"
    "s_waitcnt lgkmcnt(3)\n"
    "ds_read_b128 v[232:235], v25 offset:33792\n"
    "v_mfma_f32_16x16x32_bf16 v[216:219], a[0:3], a[208:211], v[216:219]\n"
    "ds_read_b128 v[236:239], v26 offset:33792\n"
    "v_mfma_f32_16x16x32_bf16 v[216:219], a[8:11], a[212:215], v[216:219]\n"
    "ds_read_b128 v[240:243], v27 offset:33792\n"
    "v_mfma_f32_16x16x32_bf16 v[216:219], a[16:19], a[216:219], v[216:219]\n"
    "ds_read_b128 v[244:247], v28 offset:33792\n"
    "v_mfma_f32_16x16x32_bf16 v[216:219], a[24:27], a[220:223], v[216:219]\n"
    "v_mfma_f32_16x16x32_bf16 v[220:223], a[28:31], a[220:223], v[220:223]\n"
    "v_mfma_f32_16x16x32_bf16 v[220:223], a[20:23], a[216:219], v[220:223]\n"
    "v_mfma_f32_16x16x32_bf16 v[220:223], a[12:15], a[212:215], v[220:223]\n"
    "v_mfma_f32_16x16x32_bf16 v[220:223], a[4:7], a[208:211], v[220:223]\n"
    "v_mfma_f32_16x16x32_bf16 v[228:231], a[4:7], a[224:227], v[228:231]\n"
    "v_mfma_f32_16x16x32_bf16 v[228:231], a[12:15], a[228:231], v[228:231]\n"
    "v_mfma_f32_16x16x32_bf16 v[228:231], a[20:23], a[232:235], v[228:231]\n"
    "v_mfma_f32_16x16x32_bf16 v[228:231], a[28:31], a[236:239], v[228:231]\n"
    "buffer_load_dword v61, off, s[52:55], s59 offset:224 sc1\n"
    "v_mfma_f32_16x16x32_bf16 v[224:227], a[24:27], a[236:239], v[224:227]\n"
    "v_mfma_f32_16x16x32_bf16 v[224:227], a[16:19], a[232:235], v[224:227]\n"
    "v_mfma_f32_16x16x32_bf16 v[224:227], a[8:11], a[228:231], v[224:227]\n"
    "v_mfma_f32_16x16x32_bf16 v[224:227], a[0:3], a[224:227], v[224:227]\n"
    "s_waitcnt vmcnt(11)\n"
    "s_barrier\n"
    "v_mfma_f32_16x16x32_bf16 v[200:203], a[32:35], a[240:243], v[200:203]\n"
    "s_waitcnt lgkmcnt(0)\n"
    "ds_read_b128 a[208:211], v25 offset:40960\n"
    "v_mfma_f32_16x16x32_bf16 v[200:203], a[40:43], a[244:247], v[200:203]\n"
    "ds_read_b128 a[212:215], v26 offset:40960\n"
    "v_mfma_f32_16x16x32_bf16 v[200:203], a[48:51], a[248:251], v[200:203]\n"
    "ds_read_b128 a[216:219], v27 offset:40960\n"
    "v_mfma_f32_16x16x32_bf16 v[200:203], a[56:59], a[252:255], v[200:203]\n"
    "ds_read_b128 a[220:223], v28 offset:40960\n"
    "v_mfma_f32_16x16x32_bf16 v[204:207], a[60:63], a[252:255], v[204:207]\n"
    "ds_read_b128 a[224:227], v25 offset:41984\n"
    "v_mfma_f32_16x16x32_bf16 v[204:207], a[52:55], a[248:251], v[204:207]\n"
    "ds_read_b128 a[228:231], v26 offset:41984\n"
    "v_mfma_f32_16x16x32_bf16 v[204:207], a[44:47], a[244:247], v[204:207]\n"
    "ds_read_b128 a[232:235], v27 offset:41984\n"
    "v_mfma_f32_16x16x32_bf16 v[204:207], a[36:39], a[240:243], v[204:207]\n"
    "ds_read_b128 a[236:239], v28 offset:41984\n"
    "v_mfma_f32_16x16x32_bf16 v[212:215], a[36:39], v[232:235], v[212:215]\n"
    "ds_read_b128 a[240:243], v29 offset:24576\n"
    "v_mfma_f32_16x16x32_bf16 v[212:215], a[44:47], v[236:239], v[212:215]\n"
    "ds_read_b128 a[244:247], v30 offset:24576\n"
    "v_mfma_f32_16x16x32_bf16 v[212:215], a[52:55], v[240:243], v[212:215]\n"
    "ds_read_b128 a[248:251], v31 offset:24576\n"
    "v_mfma_f32_16x16x32_bf16 v[212:215], a[60:63], v[244:247], v[212:215]\n"
    "s_waitcnt vmcnt(7)\n"
    "v_mfma_f32_16x16x32_bf16 v[200:203], v[94:97], v[108:111], v[200:203]\n"
    "v_mfma_f32_16x16x32_bf16 v[204:207], v[92:95], v[108:111], v[204:207]\n"
    "s_waitcnt vmcnt(6)\n"
    "v_mfma_f32_16x16x32_bf16 v[208:211], v[94:97], v[116:119], v[208:211]\n"
    "v_mfma_f32_16x16x32_bf16 v[212:215], v[92:95], v[116:119], v[212:215]\n"
    "s_waitcnt vmcnt(5)\n"
    "v_mfma_f32_16x16x32_bf16 v[216:219], v[94:97], v[124:127], v[216:219]\n"
    "v_mfma_f32_16x16x32_bf16 v[220:223], v[92:95], v[124:127], v[220:223]\n"
    "s_waitcnt vmcnt(4)\n"
    "v_mfma_f32_16x16x32_bf16 v[224:227], v[94:97], v[132:135], v[224:227]\n"
    "v_mfma_f32_16x16x32_bf16 v[228:231], v[92:95], v[132:135], v[228:231]\n"
    "ds_read_b128 a[252:255], v32 offset:24576\n"
    "v_mfma_f32_16x16x32_bf16 v[208:211], a[56:59], v[244:247], v[208:211]\n"
    "v_mfma_f32_16x16x32_bf16 v[208:211], a[48:51], v[240:243], v[208:211]\n"
    "v_mfma_f32_16x16x32_bf16 v[208:211], a[40:43], v[236:239], v[208:211]\n"
    "v_mfma_f32_16x16x32_bf16 v[208:211], a[32:35], v[232:235], v[208:211]\n"
    "s_waitcnt lgkmcnt(3)\n"
    "ds_read_b128 v[232:235], v29 offset:25600\n"
    "v_mfma_f32_16x16x32_bf16 v[200:203], a[160:163], a[208:211], v[200:203]\n"
    "ds_read_b128 v[236:239], v30 offset:25600\n"
    "v_mfma_f32_16x16x32_bf16 v[200:203], a[168:171], a[212:215], v[200:203]\n"
    "ds_read_b128 v[240:243], v31 offset:25600\n"
    "v_mfma_f32_16x16x32_bf16 v[200:203], a[176:179], a[216:219], v[200:203]\n"
    "ds_read_b128 v[244:247], v32 offset:25600\n"
    "v_mfma_f32_16x16x32_bf16 v[200:203], a[184:187], a[220:223], v[200:203]\n"
    "v_mfma_f32_16x16x32_bf16 v[204:207], a[188:191], a[220:223], v[204:207]\n"
    "v_mfma_f32_16x16x32_bf16 v[204:207], a[180:183], a[216:219], v[204:207]\n"
    "v_mfma_f32_16x16x32_bf16 v[204:207], a[172:175], a[212:215], v[204:207]\n"
    "s_waitcnt vmcnt(1)\n"
    "v_cmp_ne_u32 vcc, 1, v60\n"
    "v_mfma_f32_16x16x32_bf16 v[204:207], a[164:167], a[208:211], v[204:207]\n"
    "s_cbranch_vccnz .Lwf1_b%=\n"
    ".Lokf1_b%=:\n"
    "s_add_u32 s62, s98, 16384\n"
    "v_mfma_f32_16x16x32_bf16 v[212:215], a[164:167], a[224:227], v[212:215]\n"
    "buffer_load_dwordx4 v[140:143], v53, s[52:55], s62 offen offset:0 sc1\n"
    "v_mfma_f32_16x16x32_bf16 v[212:215], a[172:175], a[228:231], v[212:215]\n"
    "buffer_load_dwordx4 v[148:151], v53, s[52:55], s62 offen offset:1024 sc1\n"
    "v_mfma_f32_16x16x32_bf16 v[212:215], a[180:183], a[232:235], v[212:215]\n"
    "buffer_load_dwordx4 v[156:159], v53, s[52:55], s62 offen offset:2048 sc1\n"
    "v_mfma_f32_16x16x32_bf16 v[212:215], a[188:191], a[236:239], v[212:215]\n"
    "buffer_load_dwordx4 v[164:167], v53, s[52:55], s62 offen offset:3072 sc1\n"
    "v_mfma_f32_16x16x32_bf16 v[208:211], a[184:187], a[236:239], v[208:211]\n"
    "v_mfma_f32_16x16x32_bf16 v[208:211], a[176:179], a[232:235], v[208:211]\n"
    "v_mfma_f32_16x16x32_bf16 v[208:211], a[168:171], a[228:231], v[208:211]\n"
    "v_mfma_f32_16x16x32_bf16 v[208:211], a[160:163], a[224:227], v[208:211]\n"
    "s_barrier\n"
    "v_mfma_f32_16x16x32_bf16 v[216:219], a[160:163], a[240:243], v[216:219]\n"
    "s_waitcnt lgkmcnt(0)\n"
    "ds_read_b128 a[208:211], v25 offset:49152\n"
    "v_mfma_f32_16x16x32_bf16 v[216:219], a[168:171], a[244:247], v[216:219]\n"
    "ds_read_b128 a[212:215], v26 offset:49152\n"
    "v_mfma_f32_16x16x32_bf16 v[216:219], a[176:179], a[248:251], v[216:219]\n"
    "ds_read_b128 a[216:219], v27 offset:49152\n"
    "v_mfma_f32_16x16x32_bf16 v[216:219], a[184:187], a[252:255], v[216:219]\n"
    "ds_read_b128 a[220:223], v28 offset:49152\n"
    "v_mfma_f32_16x16x32_bf16 v[220:223], a[188:191], a[252:255], v[220:223]\n"
    "ds_read_b128 a[224:227], v25 offset:50176\n"
    "v_mfma_f32_16x16x32_bf16 v[220:223], a[180:183], a[248:251], v[220:223]\n"
    "ds_read_b128 a[228:231], v26 offset:50176\n"
    "v_mfma_f32_16x16x32_bf16 v[220:223], a[172:175], a[244:247], v[220:223]\n"
    "ds_read_b128 a[232:235], v27 offset:50176\n"
    "v_mfma_f32_16x16x32_bf16 v[220:223], a[164:167], a[240:243], v[220:223]\n"
    "s_waitcnt vmcnt(4)\n"
    "ds_read_b128 a[236:239], v28 offset:50176\n"
    "v_cmp_ne_u32 vcc, 1, v61\n"
    "v_mfma_f32_16x16x32_bf16 v[228:231], a[164:167], v[232:235], v[228:231]\n"
    "s_cbranch_vccnz .Lwf2_b%=\n"
    ".Lokf2_b%=:\n"
    "ds_read_b128 a[240:243], v25 offset:57344\n"
    "s_add_u32 s62, s98, 32768\n"
    "v_mfma_f32_16x16x32_bf16 v[228:231], a[172:175], v[236:239], v[228:231]\n"
    "ds_read_b128 a[244:247], v26 offset:57344\n"
    "buffer_load_dwordx4 v[172:175], v53, s[52:55], s62 offen offset:0 sc1\n"
    "v_mfma_f32_16x16x32_bf16 v[228:231], a[180:183], v[240:243], v[228:231]\n"
    "ds_read_b128 a[248:251], v27 offset:57344\n"
    "buffer_load_dwordx4 v[180:183], v53, s[52:55], s62 offen offset:1024 sc1\n"
    "v_mfma_f32_16x16x32_bf16 v[228:231], a[188:191], v[244:247], v[228:231]\n"
    "ds_read_b128 a[252:255], v28 offset:57344\n"
    "buffer_load_dwordx4 v[188:191], v53, s[52:55], s62 offen offset:2048 sc1\n"
    "v_mfma_f32_16x16x32_bf16 v[224:227], a[184:187], v[244:247], v[224:227]\n"
    "buffer_load_dwordx4 v[196:199], v53, s[52:55], s62 offen offset:3072 sc1\n"
    "v_mfma_f32_16x16x32_bf16 v[224:227], a[176:179], v[240:243], v[224:227]\n"
    "v_mfma_f32_16x16x32_bf16 v[224:227], a[168:171], v[236:239], v[224:227]\n"
    "v_mfma_f32_16x16x32_bf16 v[224:227], a[160:163], v[232:235], v[224:227]\n"
    "s_waitcnt lgkmcnt(3)\n"
    "ds_read_b128 v[232:235], v25 offset:58368\n"
    "v_mfma_f32_16x16x32_bf16 v[200:203], a[128:131], a[208:211], v[200:203]\n"
    "ds_read_b128 v[236:239], v26 offset:58368\n"
    "v_mfma_f32_16x16x32_bf16 v[200:203], a[136:139], a[212:215], v[200:203]\n"
    "ds_read_b128 v[240:243], v27 offset:58368\n"
    "v_mfma_f32_16x16x32_bf16 v[200:203], a[144:147], a[216:219], v[200:203]\n"
    "ds_read_b128 v[244:247], v28 offset:58368\n"
    "v_mfma_f32_16x16x32_bf16 v[200:203], a[152:155], a[220:223], v[200:203]\n"
    "v_mfma_f32_16x16x32_bf16 v[204:207], a[156:159], a[220:223], v[204:207]\n"
    "v_mfma_f32_16x16x32_bf16 v[204:207], a[148:151], a[216:219], v[204:207]\n"
    "v_mfma_f32_16x16x32_bf16 v[204:207], a[140:143], a[212:215], v[204:207]\n"
    "v_mfma_f32_16x16x32_bf16 v[204:207], a[132:135], a[208:211], v[204:207]\n"
    "v_mfma_f32_16x16x32_bf16 v[212:215], a[132:135], a[224:227], v[212:215]\n"
    "v_mfma_f32_16x16x32_bf16 v[212:215], a[140:143], a[228:231], v[212:215]\n"
    "v_mfma_f32_16x16x32_bf16 v[212:215], a[148:151], a[232:235], v[212:215]\n"
    "v_mfma_f32_16x16x32_bf16 v[212:215], a[156:159], a[236:239], v[212:215]\n"
    "v_mfma_f32_16x16x32_bf16 v[208:211], a[152:155], a[236:239], v[208:211]\n"
    "v_mfma_f32_16x16x32_bf16 v[208:211], a[144:147], a[232:235], v[208:211]\n"
    "v_mfma_f32_16x16x32_bf16 v[208:211], a[136:139], a[228:231], v[208:211]\n"
    "v_mfma_f32_16x16x32_bf16 v[208:211], a[128:131], a[224:227], v[208:211]\n"
    "s_barrier\n"
    "v_mfma_f32_16x16x32_bf16 v[216:219], a[128:131], a[240:243], v[216:219]\n"
    "s_waitcnt lgkmcnt(0)\n"
    "v_mfma_f32_16x16x32_bf16 v[216:219], a[136:139], a[244:247], v[216:219]\n"
    "v_mfma_f32_16x16x32_bf16 v[216:219], a[144:147], a[248:251], v[216:219]\n"
    "v_mfma_f32_16x16x32_bf16 v[216:219], a[152:155], a[252:255], v[216:219]\n"
    "v_mfma_f32_16x16x32_bf16 v[220:223], a[156:159], a[252:255], v[220:223]\n"
    "v_mfma_f32_16x16x32_bf16 v[220:223], a[148:151], a[248:251], v[220:223]\n"
    "v_mfma_f32_16x16x32_bf16 v[220:223], a[140:143], a[244:247], v[220:223]\n"
    "v_mfma_f32_16x16x32_bf16 v[220:223], a[132:135], a[240:243], v[220:223]\n"
    "v_mfma_f32_16x16x32_bf16 v[228:231], a[132:135], v[232:235], v[228:231]\n"
    "v_mfma_f32_16x16x32_bf16 v[228:231], a[140:143], v[236:239], v[228:231]\n"
    "v_mfma_f32_16x16x32_bf16 v[228:231], a[148:151], v[240:243], v[228:231]\n"
    "v_mfma_f32_16x16x32_bf16 v[228:231], a[156:159], v[244:247], v[228:231]\n"
    "v_mfma_f32_16x16x32_bf16 v[224:227], a[152:155], v[244:247], v[224:227]\n"
    "v_mfma_f32_16x16x32_bf16 v[224:227], a[144:147], v[240:243], v[224:227]\n"
    "v_mfma_f32_16x16x32_bf16 v[224:227], a[136:139], v[236:239], v[224:227]\n"
    "v_mfma_f32_16x16x32_bf16 v[224:227], a[128:131], v[232:235], v[224:227]\n"
    "s_waitcnt vmcnt(7)\n"
    "v_mfma_f32_16x16x32_bf16 v[200:203], v[94:97], v[140:143], v[200:203]\n"
    "v_mfma_f32_16x16x32_bf16 v[204:207], v[92:95], v[140:143], v[204:207]\n"
    "s_waitcnt vmcnt(6)\n"
    "v_mfma_f32_16x16x32_bf16 v[208:211], v[94:97], v[148:151], v[208:211]\n"
    "v_mfma_f32_16x16x32_bf16 v[212:215], v[92:95], v[148:151], v[212:215]\n"
    "s_waitcnt vmcnt(5)\n"
    "v_mfma_f32_16x16x32_bf16 v[216:219], v[94:97], v[156:159], v[216:219]\n"
    "v_mfma_f32_16x16x32_bf16 v[220:223], v[92:95], v[156:159], v[220:223]\n"
    "s_waitcnt vmcnt(4)\n"
    "v_mfma_f32_16x16x32_bf16 v[224:227], v[94:97], v[164:167], v[224:227]\n"
    "v_mfma_f32_16x16x32_bf16 v[228:231], v[92:95], v[164:167], v[228:231]\n"
    "s_waitcnt vmcnt(3)\n"
    "v_mfma_f32_16x16x32_bf16 v[200:203], v[94:97], v[172:175], v[200:203]\n"
    "s_lshl2_add_u32 s62, s56, 131072\n"
    "v_lshlrev_b32 v74, 4, v1\n"
    "v_xor_b32 v75, s15, v1\n"
    "v_mfma_f32_16x16x32_bf16 v[204:207], v[92:95], v[172:175], v[204:207]\n"
    "s_lshl_b32 s63, s56, 8\n"
    "v_lshl_add_u32 v75, v75, 4, s62\n"
    "s_add_u32 s63, s63, 131072\n"
    "v_add_u32 v74, s63, v74\n"
    "s_waitcnt vmcnt(2)\n"
    "v_mfma_f32_16x16x32_bf16 v[208:211], v[94:97], v[180:183], v[208:211]\n"
    "v_cvt_pk_bf16_f32 v136, v200, v201\n"
    "v_cvt_pk_bf16_f32 v137, v202, v203\n"
    "v_mfma_f32_16x16x32_bf16 v[212:215], v[92:95], v[180:183], v[212:215]\n"
    "v_cvt_pk_bf16_f32 v138, v204, v205\n"
    "v_cvt_pk_bf16_f32 v139, v206, v207\n"
    "v_pk_max_i16 v136, v136, 0\n"
    "s_waitcnt vmcnt(1)\n"
    "v_mfma_f32_16x16x32_bf16 v[216:219], v[94:97], v[188:191], v[216:219]\n"
    "v_pk_max_i16 v137, v137, 0\n"
    "v_pk_max_i16 v138, v138, 0\n"
    "v_pk_max_i16 v139, v139, 0\n"
    "v_mfma_f32_16x16x32_bf16 v[220:223], v[92:95], v[188:191], v[220:223]\n"
    "v_cvt_pk_bf16_f32 v144, v208, v209\n"
    "v_cvt_pk_bf16_f32 v145, v210, v211\n"
    "v_cvt_pk_bf16_f32 v146, v212, v213\n"
    "s_waitcnt vmcnt(0)\n"
    "v_mfma_f32_16x16x32_bf16 v[224:227], v[94:97], v[196:199], v[224:227]\n"
    "v_cvt_pk_bf16_f32 v147, v214, v215\n"
    "v_pk_max_i16 v144, v144, 0\n"
    "v_pk_max_i16 v145, v145, 0\n"
    "v_mfma_f32_16x16x32_bf16 v[228:231], v[92:95], v[196:199], v[228:231]\n"
    "v_pk_max_i16 v146, v146, 0\n"
    "v_pk_max_i16 v147, v147, 0\n"
    "v_cvt_pk_bf16_f32 v152, v216, v217\n"
    "v_mfma_f32_16x16x32_bf16 v[100:103], v[104:107], v[136:139], v[100:103]\n"
    "v_cvt_pk_bf16_f32 v153, v218, v219\n"
    "v_cvt_pk_bf16_f32 v154, v220, v221\n"
    "v_cvt_pk_bf16_f32 v155, v222, v223\n"
    "v_mfma_f32_16x16x32_bf16 v[100:103], v[112:115], v[144:147], v[100:103]\n"
    "v_pk_max_i16 v152, v152, 0\n"
    "v_pk_max_i16 v153, v153, 0\n"
    "v_pk_max_i16 v154, v154, 0\n"
    "v_pk_max_i16 v155, v155, 0\n"
    "v_cvt_pk_bf16_f32 v160, v224, v225\n"
    "v_cvt_pk_bf16_f32 v161, v226, v227\n"
    "v_mfma_f32_16x16x32_bf16 v[100:103], v[120:123], v[152:155], v[100:103]\n"
    "v_cvt_pk_bf16_f32 v162, v228, v229\n"
    "v_cvt_pk_bf16_f32 v163, v230, v231\n"
    "v_pk_max_i16 v160, v160, 0\n"
    "v_pk_max_i16 v161, v161, 0\n"
    "v_pk_max_i16 v162, v162, 0\n"
    "v_pk_max_i16 v163, v163, 0\n"
    "s_nop 1\n"
    "v_mfma_f32_16x16x32_bf16 v[100:103], v[128:131], v[160:163], v[100:103]\n"
    "s_xor_b32 s62, s58, s56\n"
    "s_lshl_b32 s62, s62, 11\n"
    "s_xor_b32 s62, s62, s39\n"
    "v_lshl_add_u32 v62, v1, 3, s62\n"
    "s_lshl4_add_u32 s63, s56, s99\n"
    "v_add_lshl_u32 v63, v1, s63, 1\n"
    "s_nop 1\n"
    "ds_write_b32 v75, v100\n"
    "s_mov_b64 exec, 0xffff\n"
    "s_mov_b32 s63, 0x37800080\n"
    "s_waitcnt lgkmcnt(0)\n"
    "s_barrier\n"
    "ds_read_b128 v[68:71], v74\n"
    "s_waitcnt lgkmcnt(0)\n"
    "v_pk_add_f32 v[68:69], v[68:69], v[70:71]\n"
    "v_add_f32 v100, v68, v69\n"
    "v_med3_f32 v100, v100, -s101, s101\n"
    "v_fract_f32 v68, v100\n"
    "v_cvt_flr_i32_f32 v71, v100\n"
    "v_cvt_pknorm_u16_f32 v70, s63, v68\n"
    "global_atomic_add_x2 v[72:73], v62, v[70:71], s[18:19] sc0 sc1\n"
    "s_mov_b32 s62, 0x2f800000\n"
    "s_waitcnt vmcnt(0)\n"
    "v_add_co_u32 v70, vcc, v70, v72\n"
    "v_and_b32 v72, 15, v72\n"
    "v_cvt_f32_u32 v70, v70\n"
    "v_addc_co_u32 v71, vcc, v71, v73, vcc\n"
    "v_cmpx_eq_u32 exec, 3, v72\n"
    "v_cvt_f32_i32 v71, v71\n"
    "v_fma_f32 v100, v70, s62, v71\n"
    "v_mul_f32 v100, 0xbfb8aa3b, v100\n"
    "v_exp_f32 v100, v100\n"
    "s_nop 0\n"
    "v_add_f32 v100, 1.0, v100\n"
    "v_rcp_f32 v100, v100\n"
    "s_nop 0\n"
    "v_cvt_pk_bf16_f32 v100, v100, v100\n"
    "global_store_short v63, v100, s[32:33]\n"
    "s_mov_b64 exec, -1\n"
    ".Lend_b%=:\n"
    "s_endpgm\n"
    ".Lwr_b%=:\n"
    "s_add_u32 s100, s100, 1\n"
    "s_cmp_gt_u32 s100, 100000\n"
    "s_cbranch_scc1 .Lokr_b%=\n"
    "buffer_store_dword v57, v54, s[52:55], 0 offen sc1\n"
    "buffer_load_dword v56, v55, s[52:55], s39 offen offset:128 sc1\n"
    "s_waitcnt vmcnt(0)\n"
    "v_cmp_ne_u32 vcc, 1, v56\n"
    "s_cbranch_vccnz .Lwr_b%=\n"
    "s_branch .Lokr_b%=\n"
    ".Lwf0_b%=:\n"
    "s_add_u32 s100, s100, 1\n"
    "s_cmp_gt_u32 s100, 100000\n"
    "s_cbranch_scc1 .Lokf0_b%=\n"
    "buffer_load_dword v59, off, s[52:55], s59 offset:192 sc1\n"
    "s_waitcnt vmcnt(0)\n"
    "v_cmp_ne_u32 vcc, 1, v59\n"
    "s_cbranch_vccnz .Lwf0_b%=\n"
    "s_branch .Lokf0_b%=\n"
    ".Lwf1_b%=:\n"
    "s_add_u32 s100, s100, 1\n"
    "s_cmp_gt_u32 s100, 100000\n"
    "s_cbranch_scc1 .Lokf1_b%=\n"
    "buffer_load_dword v60, off, s[52:55], s59 offset:208 sc1\n"
    "s_waitcnt vmcnt(0)\n"
    "v_cmp_ne_u32 vcc, 1, v60\n"
    "s_cbranch_vccnz .Lwf1_b%=\n"
    "s_branch .Lokf1_b%=\n"
    ".Lwf2_b%=:\n"
    "s_add_u32 s100, s100, 1\n"
    "s_cmp_gt_u32 s100, 100000\n"
    "s_cbranch_scc1 .Lokf2_b%=\n"
    "buffer_load_dword v61, off, s[52:55], s59 offset:224 sc1\n"
    "s_waitcnt vmcnt(0)\n"
    "v_cmp_ne_u32 vcc, 1, v61\n"
    "s_cbranch_vccnz .Lwf2_b%=\n"
    "s_branch .Lokf2_b%=\n"
    :
    : "s"(__builtin_amdgcn_kernarg_segment_ptr()), "s"(blockIdx.x), "v"(threadIdx.x), "s"(lbase)
    : "v1", "v2", "v3", "v4", "v5", "v6", "v7", "v8", "v9", "v10", "v11", "v12", "v13", "v14", "v15", "v16", "v17", "v18", "v19", "v20", "v21", "v22", "v23", "v24", "v25", "v26", "v27", "v28", "v29", "v30", "v31", "v32", "v33", "v34", "v35", "v36", "v37", "v38", "v39", "v40", "v41", "v42", "v43", "v44", "v45", "v46", "v47", "v48", "v49", "v50", "v51", "v52", "v53", "v54", "v55", "v56", "v57", "v58", "v59", "v60", "v61", "v62", "v63", "v64", "v65", "v66", "v67", "v68", "v69", "v70", "v71", "v72", "v73", "v74", "v75", "v76", "v77", "v78", "v79", "v80", "v81", "v82", "v83", "v84", "v85", "v86", "v87", "v88", "v89", "v90", "v91", "v92", "v93", "v94", "v95", "v96", "v97", "v98", "v99", "v100", "v101", "v102", "v103", "v104", "v105", "v106", "v107", "v108", "v109", "v110", "v111", "v112", "v113", "v114", "v115", "v116", "v117", "v118", "v119", "v120", "v121", "v122", "v123", "v124", "v125", "v126", "v127", "v128", "v129", "v130", "v131", "v132", "v133", "v134", "v135", "v136", "v137", "v138", "v139", "v140", "v141", "v142", "v143", "v144", "v145", "v146", "v147", "v148", "v149", "v150", "v151", "v152", "v153", "v154", "v155", "v156", "v157", "v158", "v159", "v160", "v161", "v162", "v163", "v164", "v165", "v166", "v167", "v168", "v169", "v170", "v171", "v172", "v173", "v174", "v175", "v176", "v177", "v178", "v179", "v180", "v181", "v182", "v183", "v184", "v185", "v186", "v187", "v188", "v189", "v190", "v191", "v192", "v193", "v194", "v195", "v196", "v197", "v198", "v199", "v200", "v201", "v202", "v203", "v204", "v205", "v206", "v207", "v208", "v209", "v210", "v211", "v212", "v213", "v214", "v215", "v216", "v217", "v218", "v219", "v220", "v221", "v222", "v223", "v224", "v225", "v226", "v227", "v228", "v229", "v230", "v231", "v232", "v233", "v234", "v235", "v236", "v237", "v238", "v239", "v240", "v241", "v242", "v243", "v244", "v245", "v246", "v247", "a0", "a1", "a2", "a3", "a4", "a5", "a6", "a7", "a8", "a9", "a10", "a11", "a12", "a13", "a14", "a15", "a16", "a17", "a18", "a19", "a20", "a21", "a22", "a23", "a24", "a25", "a26", "a27", "a28", "a29", "a30", "a31", "a32", "a33", "a34", "a35", "a36", "a37", "a38", "a39", "a40", "a41", "a42", "a43", "a44", "a45", "a46", "a47", "a48", "a49", "a50", "a51", "a52", "a53", "a54", "a55", "a56", "a57", "a58", "a59", "a60", "a61", "a62", "a63", "a64", "a65", "a66", "a67", "a68", "a69", "a70", "a71", "a72", "a73", "a74", "a75", "a76", "a77", "a78", "a79", "a80", "a81", "a82", "a83", "a84", "a85", "a86", "a87", "a88", "a89", "a90", "a91", "a92", "a93", "a94", "a95", "a96", "a97", "a98", "a99", "a100", "a101", "a102", "a103", "a104", "a105", "a106", "a107", "a108", "a109", "a110", "a111", "a112", "a113", "a114", "a115", "a116", "a117", "a118", "a119", "a120", "a121", "a122", "a123", "a124", "a125", "a126", "a127", "a128", "a129", "a130", "a131", "a132", "a133", "a134", "a135", "a136", "a137", "a138", "a139", "a140", "a141", "a142", "a143", "a144", "a145", "a146", "a147", "a148", "a149", "a150", "a151", "a152", "a153", "a154", "a155", "a156", "a157", "a158", "a159", "a160", "a161", "a162", "a163", "a164", "a165", "a166", "a167", "a168", "a169", "a170", "a171", "a172", "a173", "a174", "a175", "a176", "a177", "a178", "a179", "a180", "a181", "a182", "a183", "a184", "a185", "a186", "a187", "a188", "a189", "a190", "a191", "a192", "a193", "a194", "a195", "a196", "a197", "a198", "a199", "a200", "a201", "a202", "a203", "a204", "a205", "a206", "a207", "a208", "a209", "a210", "a211", "a212", "a213", "a214", "a215", "a216", "a217", "a218", "a219", "a220", "a221", "a222", "a223", "a224", "a225", "a226", "a227", "a228", "a229", "a230", "a231", "a232", "a233", "a234", "a235", "a236", "a237", "a238", "a239", "a240", "a241", "a242", "a243", "a244", "a245", "a246", "a247", "a248", "a249", "a250", "a251", "a252", "a253", "a254", "a255", "s15", "s16", "s17", "s18", "s19", "s20", "s21", "s22", "s23", "s24", "s25", "s26", "s27", "s28", "s29", "s30", "s31", "s32", "s33", "s34", "s35", "s36", "s37", "s38", "s39", "s40", "s41", "s42", "s43", "s44", "s45", "s46", "s47", "s48", "s49", "s50", "s51", "s52", "s53", "s54", "s55", "s56", "s57", "s58", "s59", "s60", "s61", "s62", "s63", "s64", "s65", "s66", "s67", "s68", "s69", "s70", "s71", "s72", "s73", "s74", "s75", "s76", "s77", "s78", "s79", "s80", "s81", "s82", "s83", "s84", "s85", "s86", "s87", "s88", "s89", "s90", "s91", "s92", "s93", "s94", "s95", "s96", "s97", "s98", "s99", "s100", "s101", "vcc", "scc", "memory");
}

extern "C" void kernel_launch(void* const* d_in, const int* in_sizes, int n_in, void* d_out, int out_size, void* d_ws,
                              size_t ws_size, hipStream_t stream) {
  const int* idx = (const int*)d_in[0];
  const __hip_bfloat16* table = (const __hip_bfloat16*)d_in[1];
  const __hip_bfloat16* xw = (const __hip_bfloat16*)d_in[2];
  const __hip_bfloat16* ww = (const __hip_bfloat16*)d_in[3];
  const __hip_bfloat16* bw = (const __hip_bfloat16*)d_in[4];
  const __hip_bfloat16* W1 = (const __hip_bfloat16*)d_in[5];
  const __hip_bfloat16* b1 = (const __hip_bfloat16*)d_in[6];
  const __hip_bfloat16* W2 = (const __hip_bfloat16*)d_in[7];
  const __hip_bfloat16* b2 = (const __hip_bfloat16*)d_in[8];
  wd_main<<<256, 256, 0, stream>>>(idx, W1, table, (float*)d_ws, b1, W2, xw, ww, bw, b2, (__hip_bfloat16*)d_out);
}
```
